```python
import torch

def _softmax(x, axis=-1):
    return torch.softmax(x, dim=axis)

def _gelu(x):
    return torch.nn.functional.gelu(x, approximate='tanh')

def _silu(x):
    return torch.nn.functional.silu(x)

def _sigmoid(x):
    return torch.sigmoid(x)

def _layernorm(x, axis=-1, eps=1e-05):
    m = x.mean(dim=axis, keepdim=True)
    v = x.var(dim=axis, keepdim=True, unbiased=False)
    return (x - m) / torch.sqrt(v + eps)

def _scatter_add(tgt, idx, src, axis=0):
    return tgt.clone().index_add_(axis, idx, src)

def _segment_sum(src, seg, n):
    out = torch.zeros((n,) + src.shape[1:], dtype=src.dtype)
    return out.index_add_(0, seg, src)

def _pad(x, pw):
    flat = tuple((v for pair in reversed(pw) for v in pair))
    return torch.nn.functional.pad(x, flat)
kernel_name = 'adaLN_cond__memory_aug_recurrent_mem_tr__mixer_channel_mlp__prompt'

def setup_inputs():

    _w = lambda a: (lambda t: t.to(torch.bfloat16) if t.is_floating_point() else t)(torch.as_tensor(a))

    def _A_setup():
        B, S, Dm = (1, 2048, 640)
        H, Dh = (32, 192)
        M, seg = (8, 32)
        seg = min(seg, S)
        S = S // seg * seg
        return {'x': _w((lambda *s: torch.randn(*s, dtype=torch.bfloat16) * 0.05)(B, S, Dm)), 'mem0': _w((lambda *s: torch.randn(*s, dtype=torch.bfloat16) * 0.4)(B, M, Dm)), 'Wq': _w((lambda *s: torch.randn(*s, dtype=torch.bfloat16) * 1.6)(Dm, H, Dh) * Dm ** (-0.5)), 'Wk': _w((lambda *s: torch.randn(*s, dtype=torch.bfloat16) * 1.6)(Dm, H, Dh) * Dm ** (-0.5)), 'Wv': _w((lambda *s: torch.randn(*s, dtype=torch.bfloat16) * 0.4)(Dm, H, Dh) * Dm ** (-0.5)), 'Wo': _w((lambda *s: torch.randn(*s, dtype=torch.bfloat16) * 0.4)(H, Dh, Dm) * (H * Dh) ** (-0.5)), 'seg_len': seg}
    _A_in = _A_setup()

    def _B_setup():
        B, S, D, F = (1, 2048, 640, 1536)
        return {'x': _w((lambda *s: torch.randn(*s, dtype=torch.bfloat16) * 0.05)(B, S, D)), 'w1': _w((lambda *s: torch.randn(*s, dtype=torch.bfloat16) * 1.6)(D, F) * D ** (-0.5)), 'b1': _w((lambda *s: torch.randn(*s, dtype=torch.bfloat16) * 0.4)(F)), 'w2': _w((lambda *s: torch.randn(*s, dtype=torch.bfloat16) * 0.1)(F, D) * F ** (-0.5)), 'b2': _w((lambda *s: torch.randn(*s, dtype=torch.bfloat16) * 0.1)(D))}
    _B_in = _B_setup()
    return {'A_x': _A_in['x'], 'A_mem0': _A_in['mem0'], 'A_Wq': _A_in['Wq'], 'A_Wk': _A_in['Wk'], 'A_Wv': _A_in['Wv'], 'A_Wo': _A_in['Wo'], 'A_seg_len': _A_in['seg_len'], 'B_x': _B_in['x'], 'B_w1': _B_in['w1'], 'B_b1': _B_in['b1'], 'B_w2': _B_in['w2'], 'B_b2': _B_in['b2'], 'c': _w((lambda *s: torch.randn(*s, dtype=torch.bfloat16) * 0.1)(1, 640) * 8.0), 'w_mod': _w((lambda *s: torch.randn(*s, dtype=torch.bfloat16) * 0.1)(640, 6 * 640) * 640 ** (-0.5) * 8.0)}

def reference(A_x, A_mem0, A_Wq, A_Wk, A_Wv, A_Wo, A_seg_len, B_x, B_w1, B_b1, B_w2, B_b2, c, w_mod):

    def _A_ref(x, mem0, Wq, Wk, Wv, Wo, seg_len):
        B, S, Dm = x.shape
        H, Dh = (Wq.shape[1], Wq.shape[2])
        M = mem0.shape[1]
        scale = Dh ** (-0.5)
        n_seg = S // seg_len
        L = M + seg_len
        idx = torch.arange(L)
        mask = (idx[:, None] < M) | (idx[:, None] >= idx[None, :])
        mem = mem0
        outs = []
        for s in range(n_seg):
            xs = x[:, s * seg_len:(s + 1) * seg_len, :].clone()
            h = torch.concatenate([mem, xs], axis=1)
            q = torch.einsum('btd,dhe->bhte', h, Wq)
            k = torch.einsum('btd,dhe->bhte', h, Wk)
            v = torch.einsum('btd,dhe->bhte', h, Wv)
            scores = torch.einsum('bhqe,bhke->bhqk', q, k) * scale
            scores = torch.where(mask[None, None], scores, -1000000000.0)
            p = _softmax(scores, axis=-1)
            o = torch.einsum('bhqk,bhke->bhqe', p, v)
            o = torch.einsum('bhte,hed->btd', o, Wo)
            mem = o[:, :M, :].clone()
            outs.append(o[:, M:, :])
        return torch.concatenate(outs, axis=1)

    def _B_ref(x, w1, b1, w2, b2):
        y = _layernorm(x, axis=-1)
        h = _gelu(torch.einsum('bsd,df->bsf', y, w1) + b1)
        return torch.einsum('bsf,fd->bsd', h, w2) + b2
    x = A_x
    mod = (_silu(c) @ w_mod).reshape(c.shape[0], 6, 1, -1)
    sa, ca, ga, sb, cb, gb = (mod[:, 0].clone(), mod[:, 1].clone(), mod[:, 2].clone(), mod[:, 3].clone(), mod[:, 4].clone(), mod[:, 5].clone())
    h = _layernorm(x, axis=-1) * (1 + ca) + sa
    x = x + ga * _A_ref(h, A_mem0, A_Wq, A_Wk, A_Wv, A_Wo, A_seg_len)
    h = _layernorm(x, axis=-1) * (1 + cb) + sb
    x = x + gb * _B_ref(h, B_w1, B_b1, B_w2, B_b2)
    return x
```

```cpp
#include <hip/hip_runtime.h>
#include <hip/hip_bf16.h>
namespace naive {
typedef unsigned short u16;
__device__ __forceinline__ float b2f(u16 b) { return __builtin_bit_cast(float, (unsigned)b << 16); }
__device__ __forceinline__ u16 f2b(float f) { return __builtin_bit_cast(u16, (__bf16)f); }
__device__ __forceinline__ float rb(float f) { return b2f(f2b(f)); }
constexpr int S = 2048, D = 640, H = 32, E = 192, NM = 8, SEG = 32, NSEG = 64, L = 40, F = 1536, HE = H * E;

__global__ void k_mod(const u16* c, const u16* wmod, float* mod) {
  int n = blockIdx.x * 64 + threadIdx.x;
  float a = 0;
  for (int d = 0; d < D; ++d) { float x = b2f(c[d]); float s = rb(x / (1.f + __expf(-x))); a += s * b2f(wmod[(size_t)d * 6 * D + n]); }
  mod[n] = rb(a);
}
__device__ float block_sum(float v, float* sh) {
  for (int o = 32; o; o >>= 1) v += __shfl_xor(v, o);
  __syncthreads();
  if ((threadIdx.x & 63) == 0) sh[threadIdx.x >> 6] = v;
  __syncthreads();
  float r = 0; for (int i = 0; i < (int)blockDim.x / 64; ++i) r += sh[i];
  return r;
}
__global__ void k_ln(const float* in, const float* sc, const float* sh_, float* out) {
  __shared__ float sh[16];
  int r = blockIdx.x, d = threadIdx.x;
  float x = in[(size_t)r * D + d];
  float m = block_sum(x, sh) / D;
  float v = block_sum((x - m) * (x - m), sh) / D;
  float y = (x - m) / sqrtf(v + 1e-5f);
  if (sc) y = y * (1.f + sc[d]) + sh_[d];
  out[(size_t)r * D + d] = rb(y);
}
__global__ void k_cvt(const u16* in, float* out, int n) { int i = blockIdx.x * 256 + threadIdx.x; if (i < n) out[i] = b2f(in[i]); }
template <int MODE>
__global__ void k_mm(const float* A, const u16* B, float* C, int M, int N, int K, const u16* bias) {
  int n = blockIdx.x * 128 + threadIdx.x, m0 = blockIdx.y * 8;
  if (n >= N) return;
  float acc[8] = {0, 0, 0, 0, 0, 0, 0, 0};
  for (int k = 0; k < K; ++k) { float b = b2f(B[(size_t)k * N + n]);
#pragma unroll
    for (int i = 0; i < 8; ++i) acc[i] += A[(size_t)(m0 + i) * K + k] * b; }
#pragma unroll
  for (int i = 0; i < 8; ++i) {
    float v = rb(acc[i]);
    if (MODE >= 1) v = rb(v + b2f(bias[n]));
    if (MODE == 1) { float u = 0.7978845608028654f * (v + 0.044715f * v * v * v); v = rb(0.5f * v * (1.f + tanhf(u))); }
    C[(size_t)(m0 + i) * N + n] = v;
  }
}
__global__ void k_attn(const float* qm, const float* km, const float* vm, const float* qt, const float* kt, const float* vt, float* o  ) {
  __shared__ float p[L];
  int h = blockIdx.x, i = blockIdx.y, t = threadIdx.x;
  const float* q = (i < NM ? qm + (size_t)i * HE : qt + (size_t)(i - NM) * HE) + h * E;
  if (t < L) {
    const float* k = (t < NM ? km + (size_t)t * HE : kt + (size_t)(t - NM) * HE) + h * E;
    float s = 0; for (int e = 0; e < E; ++e) s += q[e] * k[e];
    s = rb(rb(s) * rb(0.07216878364870322f));
    if (!(i < NM || i >= t)) s = -1e9f;
    p[t] = s;
  }
  __syncthreads();
  float mx = -3e38f; for (int j = 0; j < L; ++j) mx = fmaxf(mx, p[j]);
  float sum = 0; for (int j = 0; j < L; ++j) sum += __expf(p[j] - mx);
  float a = 0;
  for (int j = 0; j < L; ++j) { float pj = rb(__expf(p[j] - mx) / sum); const float* v = (j < NM ? vm + (size_t)j * HE : vt + (size_t)(j - NM) * HE) + h * E; a += pj * v[t]; }
  o[(size_t)i * HE + h * E + t] = rb(a);
}
__global__ void k_res(const float* x, const float* g, const float* y, float* out, u16* outb) {
  int r = blockIdx.x, d = threadIdx.x; size_t i = (size_t)r * D + d;
  float v = rb(x[i] + rb(g[d] * y[i]));
  if (out) out[i] = v;
  if (outb) outb[i] = f2b(v);
}
struct Ws { float *mod, *x, *h, *q, *k, *v, *mem, *qm, *km, *vm, *o, *oo, *aout, *x1, *h2, *y, *t, *o2, *memall; };
static Ws carve(void* ws) {
  float* p = (float*)ws; Ws w;
  auto take = [&](size_t n) { float* r = p; p += (n + 63) / 64 * 64; return r; };
  w.mod = take(6 * D); w.x = take((size_t)S * D); w.h = take((size_t)S * D);
  w.q = take((size_t)S * HE); w.k = take((size_t)S * HE); w.v = take((size_t)S * HE);
  w.mem = take(NM * D); w.qm = take(NM * HE); w.km = take(NM * HE); w.vm = take(NM * HE); w.o = take(L * HE); w.oo = take(L * D);
  w.aout = take((size_t)S * D); w.x1 = take((size_t)S * D); w.h2 = take((size_t)S * D); w.y = take((size_t)S * D); w.t = take((size_t)S * F); w.o2 = take((size_t)S * D);
  w.memall = take((size_t)(NSEG + 1) * NM * D);
  return w;
}
static void launch(void* const* d_in, void* d_out, void* d_ws, hipStream_t st) {
  const u16 *x = (const u16*)d_in[0], *mem0 = (const u16*)d_in[1], *Wq = (const u16*)d_in[2], *Wk = (const u16*)d_in[3], *Wv = (const u16*)d_in[4], *Wo = (const u16*)d_in[5];
  const u16 *w1 = (const u16*)d_in[8], *b1 = (const u16*)d_in[9], *w2 = (const u16*)d_in[10], *b2 = (const u16*)d_in[11], *c = (const u16*)d_in[12], *wmod = (const u16*)d_in[13];
  Ws w = carve(d_ws);
  k_mod<<<6 * D / 64, 64, 0, st>>>(c, wmod, w.mod);
  k_cvt<<<S * D / 256, 256, 0, st>>>(x, w.x, S * D);
  k_cvt<<<NM * D / 256, 256, 0, st>>>(mem0, w.memall, NM * D);
  k_ln<<<S, D, 0, st>>>(w.x, w.mod + D, w.mod, w.h);
  k_mm<0><<<dim3(HE / 128, S / 8), 128, 0, st>>>(w.h, Wq, w.q, S, HE, D, nullptr);
  k_mm<0><<<dim3(HE / 128, S / 8), 128, 0, st>>>(w.h, Wk, w.k, S, HE, D, nullptr);
  k_mm<0><<<dim3(HE / 128, S / 8), 128, 0, st>>>(w.h, Wv, w.v, S, HE, D, nullptr);
  for (int s = 0; s < NSEG; ++s) {
    const float* mem = w.memall + (size_t)s * NM * D;
    k_mm<0><<<dim3(HE / 128, 1), 128, 0, st>>>(mem, Wq, w.qm, NM, HE, D, nullptr);
    k_mm<0><<<dim3(HE / 128, 1), 128, 0, st>>>(mem, Wk, w.km, NM, HE, D, nullptr);
    k_mm<0><<<dim3(HE / 128, 1), 128, 0, st>>>(mem, Wv, w.vm, NM, HE, D, nullptr);
    size_t o = (size_t)s * SEG * HE;
    k_attn<<<dim3(H, L), E, 0, st>>>(w.qm, w.km, w.vm, w.q + o, w.k + o, w.v + o, w.o);
    k_mm<0><<<dim3(D / 128, 1), 128, 0, st>>>(w.o, Wo, w.memall + (size_t)(s + 1) * NM * D, NM, D, HE, nullptr);
    k_mm<0><<<dim3(D / 128, SEG / 8), 128, 0, st>>>(w.o + (size_t)NM * HE, Wo, w.aout + (size_t)s * SEG * D, SEG, D, HE, nullptr);
  }
  k_res<<<S, D, 0, st>>>(w.x, w.mod + 2 * D, w.aout, w.x1, nullptr);
  k_ln<<<S, D, 0, st>>>(w.x1, w.mod + 4 * D, w.mod + 3 * D, w.h2);
  k_ln<<<S, D, 0, st>>>(w.h2, nullptr, nullptr, w.y);
  k_mm<1><<<dim3(F / 128, S / 8), 128, 0, st>>>(w.y, w1, w.t, S, F, D, b1);
  k_mm<2><<<dim3(D / 128, S / 8), 128, 0, st>>>(w.t, w2, w.o2, S, D, F, b2);
  k_res<<<S, D, 0, st>>>(w.x1, w.mod + 5 * D, w.o2, nullptr, (u16*)d_out);
}
}
extern "C" void kernel_launch(void* const* d_in, const int* in_sizes, int n_in, void* d_out, int out_size, void* d_ws, size_t ws_size, hipStream_t stream) {
  naive::launch(d_in, d_out, d_ws, stream);
}
```

```cpp
#include <hip/hip_runtime.h>
#include <hip/hip_bf16.h>
#include <stdint.h>

namespace {
typedef unsigned short u16;
typedef unsigned v2u __attribute__((ext_vector_type(2)));
typedef unsigned v4u __attribute__((ext_vector_type(4)));
typedef unsigned v8u __attribute__((ext_vector_type(8)));
typedef float f2 __attribute__((ext_vector_type(2)));
typedef float f4 __attribute__((ext_vector_type(4)));
typedef float f16v __attribute__((ext_vector_type(16)));
typedef short s4 __attribute__((ext_vector_type(4)));
typedef __bf16 b2 __attribute__((ext_vector_type(2)));
typedef __bf16 b8 __attribute__((ext_vector_type(8)));
typedef __bf16 b16 __attribute__((ext_vector_type(16)));
#define LAS __attribute__((address_space(3)))
#define GAS __attribute__((address_space(1)))
typedef LAS unsigned char lchar;
typedef __amdgpu_buffer_rsrc_t rsrc_t;
#define DEVI __device__ __forceinline__

constexpr int S_ = 2048, D_ = 640, H_ = 32, E_ = 192, NSEG = 64, F_ = 1536, HE = H_ * E_;
constexpr int SC1 = 16;

constexpr int OFF_CTRL = 0;
constexpr int C_TICKET = 0;
constexpr int C_DTK = 1024;
constexpr int C_NEAR = 2048;
constexpr int C_FLH = 4096;
constexpr int C_FLB = 8192;
constexpr int C_FLZ = C_FLB + 96 * 128;
constexpr int C_FLY = C_FLZ + 32 * 128;
constexpr int C_FLX = C_FLY + 80 * 128;
constexpr int C_FLT = C_FLX + 64 * 128;
static_assert(C_FLT + 48 * 128 <= 49152, "ctrl");
constexpr int C_MOD = 49152;
constexpr int OFF_HAND = 65536;
constexpr int OFF_PT = OFF_HAND + NSEG * 65536;
constexpr int OFF_HF = OFF_PT + NSEG * 81920;
constexpr int OFF_UF = OFF_HF + NSEG * 40960;
constexpr int OFF_QK = OFF_UF + 80 * 327680;
constexpr int QS = 3 * 12288, QH = NSEG * QS;
constexpr int OFF_Z = OFF_QK + H_ * QH;
constexpr int ZS = 20480, ZR = NSEG * ZS, OFF_Y = OFF_Z + 16384;
constexpr int OFF_X1 = OFF_Z + 80 * ZR;
constexpr int OFF_T = OFF_X1 + NSEG * 40960;
constexpr int WS_BYTES = OFF_T + NSEG * 98304;
static_assert(WS_BYTES + 65536 + (6 << 20) <= 256 * 1024 * 1024, "workspace");

constexpr int SPIN_CAP = 1 << 16;
#define A_PRE 8
#define A_NAP 2000
#define C_NAP 0
#define C1_HOLD 5
#define C1_NAP 6

DEVI float b2f(unsigned b) { return __builtin_bit_cast(float, b << 16); }
DEVI float blo(unsigned u) { return __builtin_bit_cast(float, u << 16); }
DEVI float bhi(unsigned u) { return __builtin_bit_cast(float, u & 0xFFFF0000u); }
DEVI unsigned pk(float a, float b) { f2 x = {a, b}; return __builtin_bit_cast(unsigned, __builtin_convertvector(x, b2)); }
DEVI float rb(float f) { return blo(pk(f, f)); }
DEVI unsigned nosent(unsigned u) { return min(u, 0xFFFFFFFEu); }
DEVI unsigned fu(float f) { return __builtin_bit_cast(unsigned, f); }
DEVI float uf(unsigned u) { return __builtin_bit_cast(float, u); }
DEVI f4 mfma32(v4u a, v4u b, f4 c) { return __builtin_amdgcn_mfma_f32_16x16x32_bf16(__builtin_bit_cast(b8, a), __builtin_bit_cast(b8, b), c, 0, 0, 0); }
DEVI f16v mfma3232(v4u a, v4u b, f16v c) { return __builtin_amdgcn_mfma_f32_32x32x16_bf16(__builtin_bit_cast(b8, a), __builtin_bit_cast(b8, b), c, 0, 0, 0); }
DEVI f4 mfma16(v2u a, v2u b, f4 c) { return __builtin_amdgcn_mfma_f32_16x16x16bf16_1k(__builtin_bit_cast(s4, a), __builtin_bit_cast(s4, b), c, 0, 0, 0); }
DEVI f4 smfmac(v4u a, v8u b, f4 c, int idx) { return __builtin_amdgcn_smfmac_f32_16x16x64_bf16(__builtin_bit_cast(b8, a), __builtin_bit_cast(b16, b), c, idx, 0, 0); }
DEVI v8u cat(v4u a, v4u b) { return __builtin_shufflevector(a, b, 0, 1, 2, 3, 4, 5, 6, 7); }
DEVI v4u lo4(v8u a) { return __builtin_shufflevector(a, a, 0, 1, 2, 3); }
DEVI v4u hi4(v8u a) { return __builtin_shufflevector(a, a, 4, 5, 6, 7); }
DEVI v2u lo2(v4u a) { return __builtin_shufflevector(a, a, 0, 1); }
DEVI v2u hi2(v4u a) { return __builtin_shufflevector(a, a, 2, 3); }
DEVI rsrc_t mkrs(const void* p, int bytes) { return __builtin_amdgcn_make_buffer_rsrc((void*)p, 0, bytes, 0x00020000); }
template <int AUX> DEVI v4u ld16(rsrc_t rs, int vo, int so) { return __builtin_amdgcn_raw_buffer_load_b128(rs, vo, so, AUX); }
template <int AUX> DEVI v2u ld8(rsrc_t rs, int vo, int so) { return __builtin_amdgcn_raw_buffer_load_b64(rs, vo, so, AUX); }
template <int AUX> DEVI unsigned ld4(rsrc_t rs, int vo, int so) { return __builtin_amdgcn_raw_buffer_load_b32(rs, vo, so, AUX); }
template <int AUX> DEVI void st16(v4u v, rsrc_t rs, int vo, int so) { __builtin_amdgcn_raw_buffer_store_b128(v, rs, vo + so, 0, AUX); }
template <int AUX> DEVI void st8(v2u v, rsrc_t rs, int vo, int so) { __builtin_amdgcn_raw_buffer_store_b64(v, rs, vo, so, AUX); }
template <int AUX> DEVI void st4(unsigned v, rsrc_t rs, int vo, int so) { __builtin_amdgcn_raw_buffer_store_b32(v, rs, vo, so, AUX); }
DEVI v4u lr16(lchar* p) { return *(LAS v4u*)p; }
DEVI f4 lrf4(lchar* p) { return *(LAS f4*)p; }
DEVI void lw16(lchar* p, v4u v) { *(LAS v4u*)p = v; }
DEVI void lwf4(lchar* p, f4 v) { *(LAS f4*)p = v; }
DEVI void lw8(lchar* p, v2u v) { *(LAS v2u*)p = v; }
DEVI v2u lr8(lchar* p) { return *(LAS v2u*)p; }
DEVI v2u lrtr(lchar* p) { return __builtin_bit_cast(v2u, __builtin_amdgcn_ds_read_tr16_b64_v4i16((LAS s4*)p)); }
template <int CTRL> DEVI float dppf(float x) { return uf((unsigned)__builtin_amdgcn_update_dpp(0, (int)fu(x), CTRL, 0xF, 0xF, true)); }
template <int CTRL> DEVI unsigned dppu(unsigned x) { return (unsigned)__builtin_amdgcn_update_dpp(0, (int)x, CTRL, 0xF, 0xF, true); }
DEVI unsigned mx3(unsigned a, unsigned b, unsigned c) { return max(max(a, b), c); }
DEVI unsigned mx4(unsigned m, v4u v) { return mx3(mx3(m, v.x, v.y), v.z, v.w); }
DEVI int lane_id() { return (int)__builtin_amdgcn_mbcnt_hi(~0u, __builtin_amdgcn_mbcnt_lo(~0u, 0u)); }
DEVI unsigned xcc_id() { return (unsigned)__builtin_amdgcn_s_getreg(20 | (3 << 11)) & 7u; }
#define BAR() do { asm volatile("s_waitcnt lgkmcnt(0)" ::: "memory"); __builtin_amdgcn_s_barrier(); asm volatile("" ::: "memory"); } while (0)
#define VMWAIT(n) asm volatile("s_waitcnt vmcnt(" #n ")" ::: "memory")
#define CFENCE() asm volatile("" ::: "memory")

#define STAMP(k)
#define ST_PARAM
#define ST_ARG
#define STAMP_INIT(slot, msk)
#define STAMP_FLUSH()
#define FST(k)
#define FST_DECL
#define FST_DONE()
struct Params {
  const u16 *x, *mem0, *Wq, *Wk, *Wv, *Wo, *w1, *b1, *w2, *b2, *c, *wmod;
  u16* out;
  unsigned char* ws;
};

DEVI int m_line(int r) {
  const int t = r >> 3, u = r & 7;
  return t < 8 ? (t >> 1) * 16384 + 5120 + (t & 1) * 1024 + u * 128 : (2 * (t - 8) + (u >> 2)) * 16384 + 5120 + 2048 + (u & 3) * 128;
}

DEVI float row_sum(float v, LAS float* red, int tid) {
  { const auto t = __builtin_amdgcn_permlane16_swap(fu(v), fu(v), false, false); v = uf(t[0]) + uf(t[1]); }
  { const auto t = __builtin_amdgcn_permlane32_swap(fu(v), fu(v), false, false); v = uf(t[0]) + uf(t[1]); }
  __syncthreads();
  if ((tid & 63) < 16) red[(tid >> 6) * 16 + (tid & 15)] = v;
  __syncthreads();
  const int r = tid & 15;
  return (red[r] + red[16 + r]) + (red[32 + r] + red[48 + r]);
}
DEVI void ln40(float (&v)[40], LAS float* red, int tid) {
  float s = 0;
#pragma unroll
  for (int i = 0; i < 40; ++i) s += v[i];
  const float m = row_sum(s, red, tid) * (1.f / D_);
  float q = 0;
#pragma unroll
  for (int i = 0; i < 40; ++i) { v[i] -= m; q += v[i] * v[i]; }
  const float var = row_sum(q, red, tid) * (1.f / D_);
  const float rs = 1.f / sqrtf(var + 1e-5f);
#pragma unroll
  for (int i = 0; i < 40; ++i) v[i] *= rs;
}

DEVI float xsum16_32(float v) {
  { const auto t = __builtin_amdgcn_permlane16_swap(fu(v), fu(v), false, false); v = uf(t[0]) + uf(t[1]); }
  { const auto t = __builtin_amdgcn_permlane32_swap(fu(v), fu(v), false, false); v = uf(t[0]) + uf(t[1]); }
  return v;
}
template <int NT> DEVI void ln_tiles(const Params& p, rsrc_t ws, lchar* L, int w, int b0) {
  LAS float* red = (LAS float*)L;
  const int tid = w * 64 + lane_id(), row = tid & 15, part = tid >> 4;
  v4u xv[NT][5], sa[5], ca[5];
#pragma unroll
  for (int n = 0; n < NT; ++n)
#pragma unroll
    for (int k = 0; k < 5; ++k) xv[n][k] = *(const GAS v4u*)(p.x + (size_t)((b0 + n) * 16 + row) * D_ + (part + 16 * k) * 8);
#pragma unroll
  for (int k = 0; k < 5; ++k) { sa[k] = ld16<0>(ws, (part + 16 * k) * 16, OFF_CTRL + C_MOD); ca[k] = ld16<0>(ws, (part + 16 * k) * 16, OFF_CTRL + C_MOD + D_ * 2); }
  __builtin_amdgcn_sched_barrier(0);
#pragma unroll
  for (int n = 0; n < NT; ++n) {
    float v[40];
#pragma unroll
    for (int k = 0; k < 5; ++k)
#pragma unroll
      for (int j = 0; j < 4; ++j) { v[k * 8 + 2 * j] = blo(xv[n][k][j]); v[k * 8 + 2 * j + 1] = bhi(xv[n][k][j]); }
    ln40(v, red, tid);
#pragma unroll
    for (int k = 0; k < 5; ++k) {
      v4u o;
#pragma unroll
      for (int j = 0; j < 4; ++j) o[j] = pk(v[k * 8 + 2 * j] * (1.f + blo(ca[k][j])) + blo(sa[k][j]), v[k * 8 + 2 * j + 1] * (1.f + bhi(ca[k][j])) + bhi(sa[k][j]));
      st16<SC1>(o, ws, OFF_HF + (b0 + n) * 20480 + (part + 16 * k) * 256 + row * 16, 0);
    }
  }
  VMWAIT(0);
  __syncthreads();
  if (tid < NT) st4<SC1>(1u, ws, OFF_CTRL + C_FLH + (b0 + tid) * 4, 0);
}

template <int N> DEVI void gather(rsrc_t ws, const int (&off)[N], int so, v4u (&v)[N]) {
  int left = SPIN_CAP;
  unsigned long long bad;
  do {
#pragma unroll
    for (int k = 0; k < N; ++k) v[k] = ld16<SC1>(ws, off[k], so);
    unsigned m = 0;
#pragma unroll
    for (int k = 0; k < N; ++k) m = mx4(m, v[k]);
    bad = __builtin_amdgcn_ballot_w64(m == 0xFFFFFFFFu);
    asm volatile("s_sub_u32 %0, %0, 1\n\ts_cbranch_scc0 1f\n\ts_endpgm\n1:" : "+s"(left)::"scc", "memory");
  } while (bad != 0ull);
}
DEVI void gather_m(rsrc_t ws, int lane, int so, v4u& a, v4u& b, v2u& c) {
  int left = SPIN_CAP;
  unsigned long long bad;
  do {
    a = ld16<SC1>(ws, lane * 16, so); b = ld16<SC1>(ws, lane * 16 + 1024, so); c = ld8<SC1>(ws, lane * 8 + 2048, so);
    bad = __builtin_amdgcn_ballot_w64(mx3(mx4(mx4(0u, a), b), c.x, c.y) == 0xFFFFFFFFu);
    asm volatile("s_sub_u32 %0, %0, 1\n\ts_cbranch_scc0 1f\n\ts_endpgm\n1:" : "+s"(left)::"scc", "memory");
  } while (bad != 0ull);
}
DEVI unsigned clk() { unsigned long long t; asm volatile("s_memtime %0\n\ts_waitcnt lgkmcnt(0)" : "=s"(t)::"memory"); return (unsigned)t; }
DEVI void nap_until(unsigned t) { while ((int)(clk() - t) < 0) {} }
DEVI unsigned wait_flags(rsrc_t ws, int off, unsigned want) {
  int left = SPIN_CAP;
  unsigned f;
  for (;;) {
    f = ld4<SC1>(ws, off, 0);
    if (__builtin_amdgcn_ballot_w64(f < want) == 0ull) break;
    asm volatile("s_sub_u32 %0, %0, 1\n\ts_cbranch_scc0 1f\n\ts_endpgm\n1:\n\ts_sleep 2" : "+s"(left)::"scc", "memory");
  }
  __builtin_amdgcn_fence(__ATOMIC_ACQUIRE, "wavefront");
  return f;
}
DEVI unsigned long long near_mask(rsrc_t ws) { return __builtin_amdgcn_ballot_w64(wait_flags(ws, OFF_CTRL + C_NEAR + lane_id() * 4, 1u) == 2u); }
DEVI int hand_base(unsigned long long nm, int s) { return OFF_HAND + s * 65536 + ((int)(nm >> s) & 1) * 8192; }

template <int V> struct IC { static constexpr int value = V; };
template <int N, int I = 0, class Fn> DEVI void static_for(Fn&& f) { if constexpr (I < N) { f(IC<I>{}); static_for<N, I + 1>(f); } }
#define SB() __builtin_amdgcn_sched_barrier(0)
#define PIN_A(x) asm volatile("" : "+a"(x))
#define PIN_V(x) asm volatile("" : "+v"(x))
#define PK4(a) pk(a[0], a[1]), pk(a[2], a[3])
#define SMF_A(acc, m, wt) asm volatile("v_smfmac_f32_16x16x64_bf16 %0, %1, %2, %3" : "+v"(acc) : "v"(m), "a"(wt), "v"(sidx))
#define SMF_V(acc, m, wt) asm volatile("v_smfmac_f32_16x16x64_bf16 %0, %1, %2, %3" : "+v"(acc) : "v"(m), "v"(wt), "v"(sidx))
#define MF32_0(d, a, b) asm volatile("v_mfma_f32_16x16x32_bf16 %0, %1, %2, 0" : "=&v"(d) : "v"(a), "v"(b))
#define MF16_C(d, a, b) asm volatile("v_mfma_f32_16x16x16_bf16 %0, %1, %2, %0" : "+v"(d) : "v"(a), "v"(b))
#define NOPS(n) asm volatile("s_nop " #n)
#define MF32_0A(d, a, b) asm volatile("v_mfma_f32_16x16x32_bf16 %0, %1, %2, 0" : "=&v"(d) : "v"(a), "a"(b))
#define MF32_CA(d, a, b) asm volatile("v_mfma_f32_16x16x32_bf16 %0, %1, %2, %0" : "+v"(d) : "v"(a), "a"(b))
#define MF32_0B(d, a, b) asm volatile("v_mfma_f32_16x16x32_bf16 %0, %1, %2, 0" : "=&v"(d) : "a"(a), "v"(b))
#define MF32_CB(d, a, b) asm volatile("v_mfma_f32_16x16x32_bf16 %0, %1, %2, %0" : "+v"(d) : "a"(a), "v"(b))
template <class FILL> DEVI void proj_seg(f4 (&acc)[3][2], const v4u (&wq)[3][20], v4u (&xa)[4][2], lchar* xb, lchar* xn, FILL fill) {
  static_for<20>([&](auto kc) __attribute__((always_inline)) {
    constexpr int ks = decltype(kc)::value;
    v4u &x0 = xa[ks & 3][0], &x1 = xa[ks & 3][1];
    f4 &a00 = acc[0][0], &a01 = acc[0][1], &a10 = acc[1][0], &a11 = acc[1][1], &a20 = acc[2][0], &a21 = acc[2][1];
    const v4u &w0 = wq[0][ks], &w1 = wq[1][ks], &w2 = wq[2][ks];
    lchar* nx = ks + 2 < 20 ? xb + (ks + 2) * 1024 : xn + (ks - 18) * 1024;
    if constexpr (ks == 0) MF32_0B(a00, w0, x0); else MF32_CB(a00, w0, x0);
    SB(); xa[(ks + 2) & 3][0] = lr16(nx); SB();
    if constexpr (ks == 0) { MF32_0B(a01, w0, x1); MF32_0B(a10, w1, x0); } else { MF32_CB(a01, w0, x1); MF32_CB(a10, w1, x0); }
    SB(); xa[(ks + 2) & 3][1] = lr16(nx + 20480); SB();
    if constexpr (ks == 0) { MF32_0B(a11, w1, x1); MF32_0B(a20, w2, x0); } else { MF32_CB(a11, w1, x1); MF32_CB(a20, w2, x0); }
    SB(); fill(kc); SB();
    if constexpr (ks == 0) MF32_0B(a21, w2, x1); else MF32_CB(a21, w2, x1);
  });
  NOPS(7);
  SB();
}
template <int NT, class PUT, class FILL> DEVI void tile_pipe(const v4u (&a0)[6], const v4u (&a1)[6], const v4u (&b)[NT][6], PUT put, FILL fill) {
  f4 c[2][2];
  v4u o;
  static_for<NT>([&](auto tc) __attribute__((always_inline)) {
    constexpr int t = decltype(tc)::value;
    f4 &c0 = c[t & 1][0], &c1 = c[t & 1][1], &p0 = c[(t + 1) & 1][0], &p1 = c[(t + 1) & 1][1];
    const v4u (&bt)[6] = b[t];
    MF32_0A(c0, a0[0], bt[0]); MF32_0A(c1, a1[0], bt[0]);
    SB(); fill(tc, IC<0>{}); SB();
    MF32_CA(c0, a0[1], bt[1]); MF32_CA(c1, a1[1], bt[1]);
    SB(); fill(tc, IC<1>{}); SB();
    MF32_CA(c0, a0[2], bt[2]);
    SB(); if constexpr (t > 0) { o.x = pk(p0[0], p0[1]); o.y = pk(p0[2], p0[3]); } SB();
    MF32_CA(c1, a1[2], bt[2]);
    SB(); if constexpr (t > 0) { o.z = pk(p1[0], p1[1]); o.w = pk(p1[2], p1[3]); } SB();
    MF32_CA(c0, a0[3], bt[3]); MF32_CA(c1, a1[3], bt[3]);
    SB(); if constexpr (t > 0) put(IC<t - 1>{}, o); SB();
    MF32_CA(c0, a0[4], bt[4]); MF32_CA(c1, a1[4], bt[4]);
    MF32_CA(c0, a0[5], bt[5]); MF32_CA(c1, a1[5], bt[5]);
  });
  NOPS(7);
  SB();
  put(IC<NT - 1>{}, v4u{PK4(c[(NT - 1) & 1][0]), PK4(c[(NT - 1) & 1][1])});
  SB();
}
DEVI v4u sel_half(int lane, int hf) {
  const int n = lane & 15, j = n & 7;
  const unsigned one = (lane >> 4) == 2 * hf + (n >> 3) ? ((j & 1) ? 0x3F800000u : 0x3F80u) : 0u;
  return v4u{(j >> 1) == 0 ? one : 0u, (j >> 1) == 1 ? one : 0u, (j >> 1) == 2 ? one : 0u, (j >> 1) == 3 ? one : 0u};
}
template <int N> DEVI void xpose(const v4u (&a)[N], const v4u (&b)[N], const v4u (&sl)[N], v4u (&o)[N]) {
  f4 c[N][2];
#pragma unroll
  for (int i = 0; i < N; ++i) { MF32_0(c[i][0], a[i], sl[i]); MF32_0(c[i][1], b[i], sl[i]); }
  NOPS(7);
#pragma unroll
  for (int i = 0; i < N; ++i) { PIN_V(c[i][0]); PIN_V(c[i][1]); }
#pragma unroll
  for (int i = 0; i < N; ++i) o[i] = v4u{PK4(c[i][0]), PK4(c[i][1])};
}
DEVI float row16_max(float x) {
  asm volatile("s_nop 1\n\tv_max_f32_dpp %0, %0, %0 quad_perm:[1,0,3,2] row_mask:0xf bank_mask:0xf\n\t"
               "s_nop 1\n\tv_max_f32_dpp %0, %0, %0 quad_perm:[2,3,0,1] row_mask:0xf bank_mask:0xf\n\t"
               "s_nop 1\n\tv_max_f32_dpp %0, %0, %0 row_half_mirror row_mask:0xf bank_mask:0xf\n\t"
               "s_nop 1\n\tv_max_f32_dpp %0, %0, %0 row_mirror row_mask:0xf bank_mask:0xf" : "+v"(x));
  return x;
}
DEVI void dma_seg(v4u srd, unsigned lds, int voff, int soff) {
  asm volatile("s_nop 4\n\ts_mov_b32 m0, %0\n\ts_nop 0\n\t"
               "buffer_load_dwordx4 %1, %2, %3 offen lds\n\tbuffer_load_dwordx4 %1, %2, %3 offen offset:1024 lds\n\t"
               "buffer_load_dwordx4 %1, %2, %3 offen offset:2048 lds\n\tbuffer_load_dwordx4 %1, %2, %3 offen offset:3072 lds"
               ::"s"(lds), "v"(voff), "s"(srd), "s"(soff) : "memory");
  asm volatile("s_mov_b32 m0, %0\n\ts_nop 0\n\t"
               "buffer_load_dwordx4 %1, %2, %3 offen lds\n\tbuffer_load_dwordx4 %1, %2, %3 offen offset:1024 lds\n\t"
               "buffer_load_dwordx4 %1, %2, %3 offen offset:2048 lds\n\tbuffer_load_dwordx4 %1, %2, %3 offen offset:3072 lds"
               ::"s"(lds + 4096), "v"(voff), "s"(srd), "s"(soff + 4096) : "memory");
  asm volatile("s_mov_b32 m0, %0\n\ts_nop 0\n\t"
               "buffer_load_dwordx4 %1, %2, %3 offen lds\n\tbuffer_load_dwordx4 %1, %2, %3 offen offset:1024 lds"
               ::"s"(lds + 8192), "v"(voff), "s"(srd), "s"(soff + 8192) : "memory");
}
DEVI void dma_1k(v4u srd, unsigned lds, int voff, int soff) {
  asm volatile("s_mov_b32 m0, %0\n\ts_nop 0\n\tbuffer_load_dwordx4 %1, %2, %3 offen lds" ::"s"(lds), "v"(voff), "s"(srd), "s"(soff) : "memory");
}
DEVI v4u mk_srd(const void* p, int bytes) { const unsigned long long a = (unsigned long long)p; return v4u{(unsigned)a, (unsigned)(a >> 32) & 0xFFFFu, (unsigned)bytes, 0x00020000u}; }
template <int N> DEVI void vmwait() { asm volatile("s_waitcnt vmcnt(%0)" ::"i"(N) : "memory"); }

template <int NG, bool SPARSE, int RSB, class SRD, class SO, class PICK, class SINK> DEVI void stream_proj(lchar* L, int lane, int w, SRD srd, SO so, PICK pick, SINK sink) {
  int vo[3];
#pragma unroll
  for (int i = 0; i < 3; ++i) {
    const int n = 64 * i + lane, row = n / 24, c = n % 24, sw = 2 * (((row >> (SPARSE ? 2 : 1)) & 1) | ((w & 1) << 1));
    vo[i] = (8 * w + row) * RSB + ((c & ~7) | ((c & 7) ^ sw)) * 16;
  }
  lchar* ra[3];
  {
    const int g = lane >> 4, q = (lane & 15) >> 2, pp = lane & 3;
    const int row = SPARSE ? 16 * (g >> 1) + 4 * (g & 1) + (q & 1) + 8 * (q >> 1) : 8 * g + q;
    const int sw = 2 * (((row >> (SPARSE ? 2 : 1)) & 1) | (((row >> 3) & 1) << 1));
#pragma unroll
    for (int t = 0; t < 3; ++t) {
      const int c = 2 * (t < 2 ? 2 * w + t : 8 + w) + (pp >> 1);
      ra[t] = L + row * 384 + ((c & ~7) | ((c & 7) ^ sw)) * 16 + (pp & 1) * 8;
    }
  }
  const unsigned lb = (unsigned)(size_t)L + w * 3072;
  auto issue = [&](auto gc) __attribute__((always_inline)) {
    constexpr int g = decltype(gc)::value;
    static_for<4>([&](auto cc) __attribute__((always_inline)) {
      constexpr int c = decltype(cc)::value;
      const int b = so(gc, cc);
#pragma unroll
      for (int i = 0; i < 3; ++i) dma_1k(srd(gc, cc), lb + ((g % 3) * 4 + c) * 12288 + i * 1024, vo[i], b);
    });
  };
  asm volatile("s_nop 4");
  static_for<3>([&](auto gc) __attribute__((always_inline)) { issue(gc); });
  static_for<NG>([&](auto gc) __attribute__((always_inline)) {
    constexpr int g = decltype(gc)::value;
    vmwait<(NG - 1 - g < 2 ? NG - 1 - g : 2) * 12>();
    BAR();
    v4u o[4][3];
    static_for<4>([&](auto cc) __attribute__((always_inline)) {
      constexpr int c = decltype(cc)::value;
      const int off = ((g % 3) * 4 + pick(gc, cc)) * 12288;
#pragma unroll
      for (int t = 0; t < 3; ++t) { const v2u x0 = lrtr(ra[t] + off), x1 = lrtr(ra[t] + off + (SPARSE ? 2 : 4) * 384); o[c][t] = v4u{x0.x, x0.y, x1.x, x1.y}; }
    });
    BAR();
    if constexpr (g + 3 < NG) issue(IC<g + 3>{});
    static_for<4>([&](auto cc) __attribute__((always_inline)) { sink(gc, cc, o[decltype(cc)::value]); });
    SB();
  });
}

DEVI void u_job(const Params& p, rsrc_t ws, lchar* L, int w, int h, int cg) {
  const int lane = lane_id(), g = lane >> 4, rho = lane & 15;
  const rsrc_t ro = mkrs(p.Wo, D_ * HE * 2);
  const v4u sv = mk_srd(p.Wv, D_ * HE * 2);
  int vo[12];
#pragma unroll
  for (int i = 0; i < 12; ++i) { const int n = 64 * i + lane, row = n / 24, c = n % 24; vo[i] = row * (HE * 2) + ((c & ~7) | ((c & 7) ^ (row >> 2))) * 16; }
  const unsigned lb = (unsigned)(size_t)L + 49152 + w * 24576;
  auto issue = [&](int n) __attribute__((always_inline)) {
    const int so = (w * 160 + n * 32) * (HE * 2) + h * (E_ * 2);
#pragma unroll
    for (int i = 0; i < 12; ++i) dma_1k(sv, lb + (n & 1) * 12288 + i * 1024, vo[i], so);
  };
  const int srow = 16 * (rho >> 3) + 4 * ((rho >> 2) & 1) + (rho & 1) + 8 * ((rho >> 1) & 1);
  lchar* ar[2];
#pragma unroll
  for (int k = 0; k < 2; ++k) ar[k] = L + 49152 + w * 24576 + srow * 384 + ((4 * k + g) ^ (srow >> 2)) * 16;
  const int prow = 8 * (rho >> 2) + (rho & 3);
  v4u ra[6][2];
#pragma unroll
  for (int ks = 0; ks < 6; ++ks) {
    const int so = (h * E_ + ks * 32) * (D_ * 2) + cg * 256 + w * 64;
    ra[ks][0] = ld16<0>(ro, prow * (D_ * 2) + g * 16, so); ra[ks][1] = ld16<0>(ro, prow * (D_ * 2) + g * 16, so + 4 * D_ * 2);
  }
  SB();
  asm volatile("s_nop 4");
  issue(0); issue(1);
  const v4u sl[2] = {sel_half(lane, 0), sel_half(lane, 1)};
  VMWAIT(0);
  __builtin_amdgcn_s_waitcnt(0x0F70);
  v4u a[5][2][6];
  auto fetch = [&](auto nc) __attribute__((always_inline)) {
    constexpr int n = decltype(nc)::value;
#pragma unroll
    for (int ks = 0; ks < 6; ++ks) { lchar* x = ar[ks & 1] + (n & 1) * 12288 + (ks >> 1) * 128; a[n][0][ks] = lr16(x); a[n][1][ks] = lr16(x + 2 * 384); }
    asm volatile("s_waitcnt lgkmcnt(0)" ::: "memory");
    SB();
  };
  fetch(IC<0>{}); fetch(IC<1>{});
  issue(2); issue(3);
#pragma unroll
  for (int ks = 0; ks < 6; ++ks) {
    const v4u x[2] = {ra[ks][0], ra[ks][0]}, y[2] = {ra[ks][1], ra[ks][1]};
    v4u o[2];
    xpose<2>(x, y, sl, o);
    lw16(L + ((w * 2) * 6 + ks) * 1024 + lane * 16, o[0]); lw16(L + ((w * 2 + 1) * 6 + ks) * 1024 + lane * 16, o[1]);
  }
  BAR();
  v4u bw[8][6];
#pragma unroll
  for (int ct = 0; ct < 8; ++ct)
#pragma unroll
    for (int ks = 0; ks < 6; ++ks) bw[ct][ks] = lr16(L + (ct * 6 + ks) * 1024 + lane * 16);
#pragma unroll
  for (int ct = 0; ct < 8; ++ct)
#pragma unroll
    for (int ks = 0; ks < 6; ++ks) PIN_A(bw[ct][ks]);
  const int dst = OFF_UF + (cg * 16 + (rho >> 3)) * 327680 + (((h >> 3) * 4 + ((h & 7) >> 1)) * 20 + w * 5) * 1024 + (g * 16 + (h & 1) * 8 + (rho & 7)) * 16;
  static_for<5>([&](auto nc) __attribute__((always_inline)) {
    constexpr int n = decltype(nc)::value;
    if constexpr (n == 1) { VMWAIT(20); fetch(IC<2>{}); issue(4); }
    if constexpr (n == 2) { VMWAIT(28); fetch(IC<3>{}); }
    if constexpr (n == 4) { VMWAIT(24); fetch(IC<4>{}); }
    tile_pipe<8>(a[n][0], a[n][1], bw, [&](auto tc, v4u v) __attribute__((always_inline)) { st16<0>(v, ws, dst, decltype(tc)::value * 2 * 327680 + n * 1024); },
                 [&](auto, auto) __attribute__((always_inline)) {});
  });
}

__global__ void __launch_bounds__(256, 1) k0_prep(Params p) {
  extern __shared__ __attribute__((aligned(16))) unsigned char smem[];
  lchar* L = (lchar*)smem;
  const rsrc_t ws = mkrs(p.ws, WS_BYTES);
  const int tid = threadIdx.x, b = blockIdx.x, w = __builtin_amdgcn_readfirstlane(tid >> 6);
  const v4u poison = {0xFFFFFFFFu, 0xFFFFFFFFu, 0xFFFFFFFFu, 0xFFFFFFFFu};
  if (b < 160) {
    u_job(p, ws, L, w, (b & 7) + 8 * (b / 40), (b >> 3) % 5);
  } else if (b < 220) {
    const int c = b - 160;
    LAS float* red = (LAS float*)L;
    const int cg = tid & 7, rg = tid >> 3;
    float acc[8] = {0, 0, 0, 0, 0, 0, 0, 0};
    v4u wv[20];
    unsigned cv[10];
#pragma unroll
    for (int i = 0; i < 20; ++i) wv[i] = *(const GAS v4u*)(p.wmod + (size_t)(rg * 20 + i) * (6 * D_) + c * 64 + cg * 8);
#pragma unroll
    for (int i = 0; i < 10; ++i) cv[i] = ((const GAS unsigned*)p.c)[rg * 10 + i];
    SB();
#pragma unroll
    for (int i = 0; i < 20; ++i) {
      const float xv = (i & 1) ? bhi(cv[i >> 1]) : blo(cv[i >> 1]);
      const float sv = rb(xv / (1.f + __expf(-xv)));
#pragma unroll
      for (int j = 0; j < 4; ++j) { acc[2 * j] += sv * blo(wv[i][j]); acc[2 * j + 1] += sv * bhi(wv[i][j]); }
    }
#pragma unroll
    for (int j = 0; j < 8; ++j) red[rg * 64 + cg * 8 + j] = acc[j];
    __syncthreads();
    if (tid < 64) {
      float s = 0;
#pragma unroll
      for (int i = 0; i < 32; ++i) s += red[i * 64 + tid];
      ((GAS u16*)(p.ws + OFF_CTRL + C_MOD))[c * 64 + tid] = (u16)pk(s, s);
    }
  } else if (b < 232) st16<0>(v4u{0, 0, 0, 0}, ws, OFF_CTRL + (b - 220) * 4096 + tid * 16, 0);
  if (b >= 160) {
#pragma unroll
    for (int i = 0; i < 24; ++i) st16<0>(poison, ws, OFF_HAND + (255 - b) * 98304 + i * 4096 + tid * 16, 0);
    if (b == 255) {
      VMWAIT(0);
      __syncthreads();
      for (int n = tid; n < 640; n += 256) {
        const int r = n >> 3, i = n & 7;
        const v4u x = *(const GAS v4u*)(p.mem0 + i * D_ + 8 * r);
        const v4u v = {nosent(x.x), nosent(x.y), nosent(x.z), nosent(x.w)};
        st16<0>(v, ws, OFF_HAND + m_line(r) + i * 16, 0);
        st16<0>(v, ws, OFF_HAND + m_line(r) + i * 16 + 8192, 0);
      }
    }
  }
}

constexpr int A_W = 0, A_M = 131072, A_PM = A_M + 10240, A_PT = A_PM + 4 * 1280, A_END = A_PT + 4 * 4096;
static_assert(A_END + 1024 <= 163840, "lds");

DEVI void role_a(const Params& p, rsrc_t ws, lchar* L, int w, int h) {
  const int lane = lane_id(), g = lane >> 4;
  STAMP_INIT(h, ST_MASK_A); STAMP(0);
  v8u wr[44];
  lchar* Lw = L + A_W + w * 32768 + lane * 16;
  {
    const v4u sq = mk_srd(p.Wq, D_ * HE * 2), sk = mk_srd(p.Wk, D_ * HE * 2);
    v4u lo[4][3], wt[16][2];
    stream_proj<10, true, HE * 2>(L, lane, w,
      [&](auto gc, auto cc) __attribute__((always_inline)) { constexpr int gi = decltype(gc)::value, c = decltype(cc)::value; return (gi < 8 ? gi >> 2 : c >> 1) ? sk : sq; },
      [&](auto gc, auto cc) __attribute__((always_inline)) {
        constexpr int gi = decltype(gc)::value, c = decltype(cc)::value, fr = gi < 8 ? 2 * c + ((gi >> 1) & 1) : 8 + (c & 1);
        return (2 * fr + (gi & 1)) * 32 * (HE * 2) + h * (E_ * 2);
      },
      [&](auto gc, auto cc) __attribute__((always_inline)) { return decltype(gc)::value < 8 ? (decltype(cc)::value ^ w) : decltype(cc)::value; },
      [&](auto gc, auto cc, v4u (&o)[3]) __attribute__((always_inline)) {
        constexpr int gi = decltype(gc)::value, c = decltype(cc)::value;
        constexpr int j = gi < 8 ? 2 * c + ((gi >> 1) & 1) : 8 + (c & 1), q = gi < 8 ? gi >> 2 : c >> 1;
        static_for<3>([&](auto tc) __attribute__((always_inline)) {
          constexpr int t = decltype(tc)::value, n = 16 * t + 2 * (j - 2) + q;
          if constexpr (j >= 2 && n % 3 == 2) wt[n / 3][gi & 1] = o[t];
          else if constexpr ((gi & 1) == 0) lo[c][t] = o[t];
          else {
            constexpr int ri = j < 2 ? j * 6 + 2 * t + q : 12 + n - (n + 1) / 3;
            v8u x = cat(lo[c][t], o[t]);
            if constexpr (ri < 32) PIN_A(x); else PIN_V(x);
            wr[ri] = x;
          }
        });
      });
#pragma unroll
    for (int i = 0; i < 16; ++i) { lw16(Lw + i * 2048, wt[i][0]); lw16(Lw + i * 2048 + 1024, wt[i][1]); }
  }
  const int sidx = (lane & 8) ? 0xEEEE : 0x4444;
  const unsigned long long nm = near_mask(ws);
  lchar* Lm_own = L + A_M + w * 2048 + lane * 16;
  lchar* Lm_half = L + A_M + 8192 + w * 512 + lane * 8;
  lchar* Lm1 = L + A_M + (w ^ 1) * 2048 + lane * 16;
  lchar* Lm2 = L + A_M + (w ^ 2) * 2048 + lane * 16;
  lchar* Lm3 = L + A_M + (w ^ 3) * 2048 + lane * 16;
  lchar* Lm8 = L + A_M + 8192 + lane * 16;
  lchar* Lpm_w = L + A_PM + w * 1280 + (lane & 7) * 160 + 32 + g * 32 + ((lane >> 3) & 1) * 16;
  lchar* Lpm_m = g >= 2 ? L + A_PM + w * 1280 + (lane & 7) * 160 + (g - 2) * 16 + ((lane >> 3) & 1) * 8 : L + A_END + w * 256 + lane * 8;
  const int u = min(lane & 15, 9);
  lchar* Lpm_r = L + A_PM + ((w & 1) * 4 + g) * 160 + u * 16;
  constexpr int OOB = 0x30000000;
  const int pst = (w < 2 && (lane & 15) < 10 && (lane & 1) == 0) ? (h >> 3) * 16384 + (u >> 1) * 1024 + (h & 7) * 128 + ((w & 1) * 4 + g) * 16 : OOB;
  const int flb = OFF_CTRL + C_FLB + (h * 3 + (lane & 1)) * 128 + w * 32;
  const int qkoff = lane * 16, qkb = OFF_QK + h * QH + w * 3072;
  v4u sel;
  {
    const int n = lane & 15, kb = 4 * (g & 1), e = (n & 7) - kb;
    const unsigned lo = e == 0 ? 0x3F80u : e == 1 ? 0x3F800000u : 0u, hi = e == 2 ? 0x3F80u : e == 3 ? 0x3F800000u : 0u;
    sel = n < 8 ? v4u{lo, hi, 0u, 0u} : v4u{0u, 0u, lo, hi};
  }
  const int ts1 = (w & 1) * 32768 + h * 1024 + lane * 16;
  const int ts2 = lane < 16 ? 65536 + (w & 1) * 8192 + h * 256 + lane * 16 : OOB;
  constexpr float CS = 0.07216878364870322f * 1.4426950408889634f;
  constexpr float NEG = -3.0e38f;
  const float lbias = (lane & 15) < 10 ? 0.f : NEG;

  VMWAIT(0); STAMP(1);
  wait_flags(ws, flb, 1u);
  STAMP(2); STAMP_FLUSH();
  unsigned prog = 0, tnap = clk();
  v4u kt[3], qt[3];
#pragma unroll
  for (int i = 0; i < 3; ++i) { kt[i] = ld16<SC1>(ws, qkoff + i * 1024, qkb + 12288); qt[i] = ld16<SC1>(ws, qkoff + i * 1024, qkb); }

#pragma unroll 1
  for (int s = 0; s < NSEG; ++s) {
    const int hb = hand_base(nm, s);
    int pst_s = pst + (s + 1 < NSEG ? hb : OOB);
    f4 acc[6];
    v8u wl[4];
#pragma unroll
    for (int i = 0; i < 6; ++i) {
      f2 a, b;
      asm volatile("v_mov_b64 %0, 0\n\tv_mov_b64 %1, 0" : "=v"(a), "=v"(b));
      acc[i] = f4{a.x, a.y, b.x, b.y};
    }
#pragma unroll
    for (int i = 0; i < 4; ++i) wl[i] = cat(lr16(Lw + i * 2048), lr16(Lw + i * 2048 + 1024));
#pragma unroll
    for (int i = 0; i < 4; ++i) PIN_V(wl[i]);
    PIN_V(pst_s);
    v4u m[10];
    {
      v2u v2;
      STAMP(0);
      nap_until(tnap);
      gather_m(ws, lane, hb + w * 16384 + 5120, m[0], m[1], v2);
      STAMP(1);
      lw16(Lm_own, m[0]); lw16(Lm_own + 1024, m[1]); lw8(Lm_half, v2);
    }
    FST_DECL FST(0)
    SB();
#pragma unroll
    for (int n = 0; n < A_PRE; ++n) SMF_A(acc[n % 6], m[n / 6], wr[n]);
    SB();
    BAR();
    STAMP(2);
    m[2] = lr16(Lm1); m[3] = lr16(Lm1 + 1024); m[4] = lr16(Lm2); m[5] = lr16(Lm2 + 1024);
    SB();
#pragma unroll
    for (int n = A_PRE; n < 12; ++n) SMF_A(acc[n % 6], m[n / 6], wr[n]);
    SB();
    m[6] = lr16(Lm3); m[7] = lr16(Lm3 + 1024); m[8] = lr16(Lm8); m[9] = lr16(Lm8 + 1024);
    FST(1)
    SB();
    f4 sM, s0, s1, ft;
    v4u pa, F01;
    v2u F2;
#pragma unroll
    for (int n = 0; n < 48; ++n) {
      const int t = n / 16, i = n % 16, tl = 2 * t + (n & 1), j = 2 + i / 2, ri = 12 + n - (n + 1) / 3;
      if (n % 3 == 2) {
        SMF_V(acc[tl], m[j], wl[(n / 3) % 4]);
        SB();
        if (n / 3 + 4 < 16) wl[(n / 3) % 4] = cat(lr16(Lw + (n / 3 + 4) * 2048), lr16(Lw + (n / 3 + 4) * 2048 + 1024));
      } else if (ri < 32) SMF_A(acc[tl], m[j], wr[ri]);
      else SMF_V(acc[tl], m[j], wr[ri]);
      SB();
      if (t >= 1) {
        if (i == 4) { pa.x = pk(acc[2 * t - 2][0], acc[2 * t - 2][1]); pa.y = pk(acc[2 * t - 2][2], acc[2 * t - 2][3]); }
        if (i == 5) { pa.z = pk(acc[2 * t - 1][0], acc[2 * t - 1][1]); pa.w = pk(acc[2 * t - 1][2], acc[2 * t - 1][3]); }
        if (i == 7) MF32_0(ft, pa, sel);
        if (i == 12) { F01[2 * t - 2] = pk(ft[0], ft[1]); F01[2 * t - 1] = pk(ft[2], ft[3]); }
      }
      if (i == 15) { FST(2 + t) }
      SB();
    }
    MF32_0(s0, kt[0], F01);
    MF32_0(s1, kt[1], F01);
    SB();
    pa.x = pk(acc[4][0], acc[4][1]); pa.y = pk(acc[4][2], acc[4][3]);
    SB();
    NOPS(3);
    SB();
    pa.z = pk(acc[5][0], acc[5][1]); pa.w = pk(acc[5][2], acc[5][3]);
    SB();
    NOPS(1);
    MF32_0(ft, pa, sel);
    MF32_0(sM, F01, F01);
    NOPS(6);
    SB();
    F2 = v2u{PK4(ft)};
    SB();
    NOPS(1);
    MF16_C(s1, hi2(kt[2]), F2);
    MF16_C(s0, lo2(kt[2]), F2);
    MF16_C(sM, F2, F2);
    NOPS(6);
    FST(5)
    SB();
#pragma unroll
    for (int i = 0; i < 4; ++i) s0[i] = uf((unsigned)__builtin_amdgcn_update_dpp((int)fu(s0[i]), (int)fu(s1[i]), 0x118, 0xF, 0xC, false));
    lwf4(Lpm_w, s0);
    lw8(Lpm_m, v2u{(unsigned)__builtin_amdgcn_update_dpp((int)fu(sM[0]), (int)fu(sM[2]), 0x118, 0xF, 0xC, false),
                   (unsigned)__builtin_amdgcn_update_dpp((int)fu(sM[1]), (int)fu(sM[3]), 0x118, 0xF, 0xC, false)});
    SB();
    STAMP(3);
    BAR();
    FST(6)
    STAMP(4);
    if (w < 2) {
      const f4 y0 = lrf4(Lpm_r), y1 = lrf4(Lpm_r + 1280), y2 = lrf4(Lpm_r + 2560), y3 = lrf4(Lpm_r + 3840);
      SB();
      const f4 x = (y0 + y1) + (y2 + y3);
      const float mc = __builtin_fmaf(row16_max(fmaxf(fmaxf(x[0], x[1]), fmaxf(x[2], x[3]))), -CS, lbias);
      float e[4];
#pragma unroll
      for (int i = 0; i < 4; ++i) { e[i] = __builtin_amdgcn_exp2f(__builtin_fmaf(x[i], CS, mc)); PIN_V(e[i]); }
      float sa = e[0] + e[1], sb = e[2] + e[3];
      PIN_V(sa);
      float sum = sa + sb;
      sum += dppf<0xB1>(sum); sum += dppf<0x4E>(sum); sum += dppf<0x141>(sum); sum += dppf<0x140>(sum);
      const float rc = __builtin_amdgcn_rcpf(sum);
      const unsigned a0 = pk(__builtin_fabsf(e[0] * rc), __builtin_fabsf(e[1] * rc)), a1 = pk(__builtin_fabsf(e[2] * rc), __builtin_fabsf(e[3] * rc));
      const unsigned n0 = dppu<0xB1>(a0), n1 = dppu<0xB1>(a1);
      st16<SC1>(v4u{a0, a1, n0, n1}, ws, pst_s, 0);
    }
    SB();
    FST(7)
    FST_DONE();
    STAMP(5);
    SB();
    {
      f4 tm0, t00, tm1, t10, t11;
      MF32_0(tm0, F01, qt[0]); MF32_0(t00, kt[0], qt[0]); MF32_0(tm1, F01, qt[1]); MF32_0(t10, kt[0], qt[1]); MF32_0(t11, kt[1], qt[1]);
      MF16_C(tm0, F2, lo2(qt[2])); MF16_C(t00, lo2(kt[2]), lo2(qt[2])); MF16_C(tm1, F2, hi2(qt[2])); MF16_C(t10, lo2(kt[2]), hi2(qt[2])); MF16_C(t11, hi2(kt[2]), hi2(qt[2]));
      NOPS(7);
      SB();
      lchar* d = L + A_PT + w * 4096 + lane * 16;
#pragma unroll
      for (int i = 0; i < 4; ++i) tm0[i] = uf(__builtin_amdgcn_permlane32_swap(fu(tm0[i]), fu(tm1[i]), false, false)[1]);
      lwf4(d, tm0); lwf4(d + 1024, t00); lwf4(d + 2048, t10); lwf4(d + 3072, t11);
    }
    BAR();
    tnap = clk() + A_NAP;
    STAMP(6);
    if (__builtin_amdgcn_ballot_w64(prog < (unsigned)min(s + 2, NSEG)) != 0ull) wait_flags(ws, flb, (unsigned)min(s + 2, NSEG));
#pragma unroll
    for (int i = 0; i < 3; ++i) { kt[i] = ld16<SC1>(ws, qkoff + i * 1024, qkb + min(s + 1, NSEG - 1) * QS + 12288); qt[i] = ld16<SC1>(ws, qkoff + i * 1024, qkb + min(s + 1, NSEG - 1) * QS); }
    prog = ld4<SC1>(ws, flb, 0);
    SB();
    if (w >= 2) {
      const int a = w - 2, q = lane & 15;
      lchar* sm = L + A_PT + a * 512 + (lane & 31) * 16;
      lchar* sd = L + A_PT + 1024 + a * 2048 + lane * 16;
      f4 r[12];
#pragma unroll
      for (int i = 0; i < 4; ++i) { r[i] = lrf4(sm + i * 4096); r[4 + i] = lrf4(sd + i * 4096); }
      f4 xf = {NEG, NEG, NEG, NEG};
      if (a) xf = (lrf4(sd - 1024) + lrf4(sd + 3072)) + (lrf4(sd + 7168) + lrf4(sd + 11264));
      f4 xm = (r[0] + r[1]) + (r[2] + r[3]), xd = (r[4] + r[5]) + (r[6] + r[7]);
      float mx = NEG;
#pragma unroll
      for (int i = 0; i < 4; ++i) {
        if (4 * g + i > q) xd[i] = NEG;
        mx = fmaxf(mx, fmaxf(xm[i], fmaxf(xd[i], xf[i])));
      }
      { const auto t = __builtin_amdgcn_permlane16_swap(fu(mx), fu(mx), false, false); mx = fmaxf(uf(t[0]), uf(t[1])); }
      { const auto t = __builtin_amdgcn_permlane32_swap(fu(mx), fu(mx), false, false); mx = fmaxf(uf(t[0]), uf(t[1])); }
      const float mc = -mx * CS;
#pragma unroll
      for (int i = 0; i < 4; ++i) {
        xm[i] = __builtin_amdgcn_exp2f(__builtin_fmaf(xm[i], CS, mc));
        xd[i] = __builtin_amdgcn_exp2f(__builtin_fmaf(xd[i], CS, mc));
        xf[i] = __builtin_amdgcn_exp2f(__builtin_fmaf(xf[i], CS, mc));
      }
      const f4 tsum = xd + xf + 0.5f * xm;
      const float rc = __builtin_amdgcn_rcpf(xsum16_32((tsum[0] + tsum[1]) + (tsum[2] + tsum[3])));
#define PKA(a, b) pk(__builtin_fabsf((a) * rc), __builtin_fabsf((b) * rc))
      const int so = OFF_PT + s * 81920;
      const v2u pd = {PKA(xd[0], xd[1]), PKA(xd[2], xd[3])}, pf = {PKA(xf[0], xf[1]), PKA(xf[2], xf[3])};
      st16<SC1>(a ? v4u{pf.x, pf.y, pd.x, pd.y} : v4u{pd.x, pd.y, pf.x, pf.y}, ws, ts1, so);
      const unsigned m0 = PKA(xm[0], xm[1]), m1 = PKA(xm[2], xm[3]);
      const auto e0 = __builtin_amdgcn_permlane16_swap(m0, m0, false, false), e1 = __builtin_amdgcn_permlane16_swap(m1, m1, false, false);
      st16<SC1>(v4u{e0[0], e1[0], e0[1], e1[1]}, ws, ts2, so);
    }
    STAMP(7); STAMP_FLUSH();
  }
}

DEVI void role_c1(const Params& p, rsrc_t ws, lchar* L, int w, int r) {
  const int lane = lane_id();
  STAMP_INIT(32 + r, ST_MASK_C); STAMP(0);
  ln_tiles<1>(p, ws, L, w, r);
  STAMP(2);
  v8u u[4][10];
#pragma unroll
  for (int nt = 0; nt < 4; ++nt)
#pragma unroll
    for (int t = 0; t < 10; ++t) {
      const int so = OFF_UF + r * 327680 + (w * 4 + nt) * 20480 + t * 2048;
      u[nt][t] = cat(ld16<0>(ws, lane * 16, so), ld16<0>(ws, lane * 16 + 1024, so));
    }
#pragma unroll
  for (int nt = 0; nt < 4; ++nt)
#pragma unroll
    for (int t = 0; t < 10; ++t) { if (nt < 3) PIN_A(u[nt][t]); else PIN_V(u[nt][t]); }
  constexpr int C_M = 0, C_PC = 10240, PCS = 1024 + 64;
  const int sidx = (lane & 8) ? 0xEEEE : 0x4444;
  const unsigned long long nm = near_mask(ws);
  const int poff[5] = {lane * 16, 1024 + lane * 16, 2048 + lane * 16, 3072 + lane * 16, 4096 + lane * 16};
  lchar* Lm_own = L + C_M + w * 2048 + lane * 16;
  lchar* Lm_half = L + C_M + 8192 + w * 512 + lane * 8;
  lchar* Lm = L + C_M + lane * 16;
  lchar* Lpc_w = L + C_PC + w * PCS + lane * 16;
  lchar* Lpc_r = L + C_PC + (lane & 3) * PCS + ((lane >> 3) + 16 * ((lane >> 2) & 1)) * 16;
  const int mst = (lane & 7) == 0 ? m_line(r) + (lane >> 3) * 16 : 0x30000000;
  const int flz = OFF_CTRL + C_FLZ + (w * 8 + (lane & 7)) * 128 + (r / 20) * 32, fly = lane == 0 ? OFF_CTRL + C_FLY + r * 128 + w * 32 : 0x30000000;
  unsigned zprog = 0;
  VMWAIT(0); STAMP(1); STAMP_FLUSH();
#pragma unroll 1
  for (int s = 0; s < NSEG; ++s) {
    const int hb = hand_base(nm, s);
    const bool more = s + 1 < NSEG;
    v4u yz[5];
    STAMP(0);
    {
      v4u v0, v1; v2u v2;
      gather_m(ws, lane, hb + w * 16384 + 5120, v0, v1, v2);
      VMWAIT(0);
      STAMP(1);
      lw16(Lm_own, v0); lw16(Lm_own + 1024, v1); lw8(Lm_half, v2);
    }
    if (__builtin_amdgcn_ballot_w64(zprog < (unsigned)s + 1u) != 0ull) zprog = wait_flags(ws, flz, (unsigned)s + 1u);
    st4<SC1>((unsigned)s, ws, fly, 0);
#pragma unroll
    for (int i = 0; i < 4; ++i) yz[1 + i] = ld16<SC1>(ws, i * 1024 + lane * 16, OFF_Z + r * ZR + s * ZS + w * 4096);
    zprog = ld4<SC1>(ws, flz, 0);
    STAMP(2);
    SB();
    BAR();
    const unsigned tnap = clk() + C_NAP;
    f4 acc[4];
#pragma unroll
    for (int i = 0; i < 4; ++i) acc[i] = f4{0, 0, 0, 0};
    v4u m[10];
#pragma unroll
    for (int t = 0; t < 10; ++t) m[t] = lr16(Lm + t * 1024);
    SB();
#pragma unroll
    for (int t = 0; t < 10; ++t)
#pragma unroll
      for (int nt = 0; nt < 4; ++nt) acc[nt] = smfmac(m[t], u[nt][t], acc[nt], sidx);
    {
      float X[4], Y[4];
#pragma unroll
      for (int i = 0; i < 4; ++i) {
        const auto a = __builtin_amdgcn_permlane32_swap(fu(acc[0][i]), fu(acc[2][i]), false, false);
        X[i] = uf(a[0]) + uf(a[1]);
        const auto b = __builtin_amdgcn_permlane32_swap(fu(acc[1][i]), fu(acc[3][i]), false, false);
        Y[i] = uf(b[0]) + uf(b[1]);
      }
      const auto q0 = __builtin_amdgcn_permlane16_swap(pk(X[0], X[1]), pk(Y[0], Y[1]), false, false);
      const auto q1 = __builtin_amdgcn_permlane16_swap(pk(X[2], X[3]), pk(Y[2], Y[3]), false, false);
      yz[0] = v4u{q0[0], q1[0], q0[1], q1[1]};
    }
    if (more) {
      int mst_s = mst + hand_base(nm, s + 1);
      PIN_V(mst_s);
      v4u pf[5];
      STAMP(3);
      nap_until(tnap);
      gather<5>(ws, poff, hb + w * 16384, pf);
      STAMP(4);
      f4 o = {0, 0, 0, 0};
#pragma unroll
      for (int i = 0; i < 5; ++i) o = mfma32(yz[i], pf[i], o);
      lwf4(Lpc_w, o);
      BAR();
      STAMP(5);
      if (w == 0) {
        f4 x = lrf4(Lpc_r) + lrf4(Lpc_r + 40 * 16);
#pragma unroll
        for (int i = 0; i < 4; ++i) x[i] += dppf<0xB1>(x[i]);
#pragma unroll
        for (int i = 0; i < 4; ++i) x[i] += dppf<0x4E>(x[i]);
        const unsigned a0 = nosent(pk(x[0], x[1])), a1 = nosent(pk(x[2], x[3]));
        const unsigned n0 = dppu<0x104>(a0), n1 = dppu<0x104>(a1);
        st16<SC1>(v4u{a0, a1, n0, n1}, ws, mst_s, 0);
      } else __builtin_amdgcn_s_sleep(C1_HOLD);
      STAMP(6);
    }
    SB();
    st16<SC1>(yz[0], ws, w * 1024 + lane * 16, OFF_Y + r * ZR + s * ZS);
    __builtin_amdgcn_s_sleep(C1_NAP);
    STAMP_FLUSH();
  }
  VMWAIT(0);
  st4<SC1>((unsigned)NSEG, ws, fly, 0);
}

DEVI void role_z(const Params& p, rsrc_t ws, lchar* L, int w, int h) {
  const int lane = lane_id(), g = lane >> 4;
  STAMP_INIT(224 + h, ST_MASK_O); STAMP(0);
  v4u wo[10][6];
  {
    const v4u so_ = mk_srd(p.Wo, D_ * HE * 2);
    int vo[10];
#pragma unroll
    for (int i = 0; i < 10; ++i) { const int n = 64 * i + lane, row = n / 80; vo[i] = (n ^ (2 * row)) * 16; }
    const int q = (lane & 15) >> 2, pp = lane & 3, row = 4 * g + q;
    const unsigned lb = (unsigned)(size_t)L + w * 10240;
    auto issue = [&](auto kc) __attribute__((always_inline)) {
      constexpr int ks = decltype(kc)::value;
#pragma unroll
      for (int i = 0; i < 10; ++i) dma_1k(so_, lb + (ks & 3) * 40960 + i * 1024, vo[i], h * E_ * D_ * 2 + ks * 40960 + w * 10240);
    };
    asm volatile("s_nop 4");
    static_for<4>([&](auto kc) __attribute__((always_inline)) { issue(kc); });
    static_for<6>([&](auto kc) __attribute__((always_inline)) {
      constexpr int ks = decltype(kc)::value;
      vmwait<(5 - ks < 3 ? 5 - ks : 3) * 10>();
      BAR();
#pragma unroll
      for (int t = 0; t < 10; ++t) {
        lchar* a = L + (ks & 3) * 40960 + row * 1280 + ((w * 20 + 2 * t + (pp >> 1)) ^ (2 * (row & 7))) * 16 + (pp & 1) * 8;
        const v2u x0 = lrtr(a), x1 = lrtr(a + 16 * 1280);
        wo[t][ks] = v4u{x0.x, x0.y, x1.x, x1.y};
      }
      BAR();
      if constexpr (ks + 4 < 6) issue(IC<ks + 4>{});
      static_for<10>([&](auto tc) __attribute__((always_inline)) {
        constexpr int t = decltype(tc)::value;
        v4u x = wo[t][ks];
        PIN_A(x);
        wo[t][ks] = x;
      });
      SB();
    });
  }
  const int flv = OFF_CTRL + C_FLB + (h * 3 + 2) * 128 + (lane & 3) * 32;
  const int flz = lane == 0 ? OFF_CTRL + C_FLZ + h * 128 + w * 32 : 0x30000000;
  const int zo = OFF_Z + (w * 20 + ((lane >> 3) & 1)) * ZR + (h >> 3) * 4096 + g * 1024 + (h & 7) * 128 + (lane & 7) * 16;
  STAMP(1); STAMP_FLUSH();
  unsigned vprog = 0, fv = 0;
  bool ready = false;
  v4u vn[12];
#pragma unroll
  for (int k = 0; k < 12; ++k) vn[k] = v4u{0, 0, 0, 0};
#pragma unroll 1
  for (int i = 0; i < NSEG; ++i) {
    STAMP(0);
    if (!ready) {
      VMWAIT(0); st4<SC1>((unsigned)i, ws, flz, 0);
      vprog = wait_flags(ws, flv, (unsigned)i + 1u);
#pragma unroll
      for (int k = 0; k < 12; ++k) vn[k] = ld16<SC1>(ws, lane * 16, OFF_QK + h * QH + i * QS + 2 * 12288 + k * 1024);
      __builtin_amdgcn_s_waitcnt(0x0F70);
    }
    v4u va[2][6];
#pragma unroll
    for (int k = 0; k < 4; ++k) { va[0][k] = vn[3 * k]; va[1][k] = vn[3 * k + 1]; }
#pragma unroll
    for (int k = 0; k < 2; ++k) {
      va[0][4 + k] = v4u{vn[6 * k + 2].x, vn[6 * k + 2].y, vn[6 * k + 5].x, vn[6 * k + 5].y};
      va[1][4 + k] = v4u{vn[6 * k + 2].z, vn[6 * k + 2].w, vn[6 * k + 5].z, vn[6 * k + 5].w};
    }
#pragma unroll
    for (int k = 0; k < 6; ++k) { PIN_V(va[0][k]); PIN_V(va[1][k]); }
    NOPS(1);
    const int nx = i + 1 < NSEG ? i + 1 : i;
    vprog = max(vprog, fv);
    ready = __builtin_amdgcn_ballot_w64(vprog < (unsigned)nx + 1u) == 0ull;
    __builtin_amdgcn_fence(__ATOMIC_ACQUIRE, "wavefront");
    STAMP(1);
    const int vso = OFF_QK + h * QH + nx * QS + 2 * 12288, zs = zo + i * ZS;
    tile_pipe<10>(va[0], va[1], wo, [&](auto tc, v4u v) __attribute__((always_inline)) { st16<SC1>(v, ws, zs, decltype(tc)::value * 2 * ZR); },
      [&](auto tc, auto kc) __attribute__((always_inline)) {
        constexpr int n = 2 * decltype(tc)::value + decltype(kc)::value;
        if constexpr (n < 12) vn[n] = ld16<SC1>(ws, lane * 16, vso + n * 1024);
        if constexpr (n == 15) fv = ld4<SC1>(ws, flv, 0);
      });
    VMWAIT(10);
    st4<SC1>((unsigned)i, ws, flz, 0);
    if (i < 2 || i == NSEG - 1) { VMWAIT(0); st4<SC1>((unsigned)i + 1u, ws, flz, 0); }
    STAMP(2); STAMP_FLUSH();
  }
}

DEVI void d_chores(const Params& p, rsrc_t ws, lchar* L, int w, int d) {
  const int lane = lane_id();
  ln_tiles<3>(p, ws, L, w, 80 + 3 * d);
  {
    unsigned best[2] = {0xFFFFFFFFu, 0xFFFFFFFFu};
    const int blk = d * 4 + w;
#pragma unroll 1
    for (int i = 0; i < 12; ++i) {
      unsigned long long c0, c1;
      asm volatile("s_memtime %0\n\ts_waitcnt lgkmcnt(0)" : "=s"(c0)::"memory");
      st4<SC1>(0u, ws, lane == 0 ? OFF_HAND + blk * 65536 + ((i >> 1) & 3) * 16384 + (i & 1) * 8192 + 7936 : 0x30000000, 0);
      VMWAIT(0);
      asm volatile("s_memtime %0\n\ts_waitcnt lgkmcnt(0)" : "=s"(c1)::"memory");
      const unsigned dt = (unsigned)(c1 - c0);
      if (i & 1) best[1] = min(best[1], dt); else best[0] = min(best[0], dt);
    }
    const unsigned nearhalf = (best[1] < best[0] ? 1u : 0u) ^ (xcc_id() >> 2);
    if (lane == 0) st4<SC1>(1u + nearhalf, ws, OFF_CTRL + C_NEAR + blk * 4, 0);
  }
}
DEVI void role_d(const Params& p, rsrc_t ws, lchar* L, int w, int q, int sidx_) {
  const int lane = lane_id(), g = lane >> 4;
  STAMP_INIT(sidx_, 0xFF); STAMP(0); STAMP_FLUSH();
  GAS unsigned* tk = (GAS unsigned*)(p.ws + OFF_CTRL + C_DTK + q * 128);
  LAS unsigned* Ltk = (LAS unsigned*)(L + 81920);
  const int flz = OFF_CTRL + C_FLZ + (w * 8 + ((lane >> 2) & 7)) * 128 + (lane & 3) * 32;
  const int can0 = lane < 16 ? OFF_PT + 65536 + (lane & 1) * 8192 + (w * 8 + (lane >> 1)) * 256 : OFF_CTRL + C_FLY + (q * 10 + min(lane - 16, 9)) * 128 + w * 32;
  const int zoff = ((lane >> 3) & 1) * ZR + g * 1024 + (lane & 7) * 16, yoff = ((lane >> 3) & 1) * ZR + g * 128 + (lane & 7) * 16;
  const rsrc_t rx = mkrs(p.x, S_ * D_ * 2);
  const int xoff = (lane & 15) * (D_ * 2) + q * 160 + g * 8;
  v2u ga[3];
#pragma unroll
  for (int k = 0; k < 3; ++k) ga[k] = ld8<0>(ws, q * 160 + g * 8, OFF_CTRL + C_MOD + 2 * D_ * 2 + (min(w + 4 * k, 9) >> 1) * 32);
  const int flx = lane == 0 ? OFF_CTRL + C_FLX + q * 16 + w * 4 : 0x30000000;
  unsigned zprog = 0;
  int buf = 0;
  v4u z[5][8];
  v2u xv[3];
  auto fetch = [&](int j, int done) __attribute__((always_inline)) {
    if (__builtin_amdgcn_ballot_w64(zprog < (unsigned)j + 1u) != 0ull) { VMWAIT(0); st4<SC1>(1u, ws, done, 0); zprog = wait_flags(ws, flz, (unsigned)j + 1u); }
    const int zb = __builtin_amdgcn_readfirstlane(OFF_Z + q * 10 * ZR + j * ZS + w * 4096);
#pragma unroll
    for (int t = 0; t < 5; ++t) {
#pragma unroll
      for (int k = 0; k < 8; ++k) z[t][k] = ld16<SC1>(ws, zoff + k * 128, zb + t * 2 * ZR);
      if (t == 1) { SB(); VMWAIT(16); st4<SC1>(1u, ws, done, 0); SB(); }
    }
#pragma unroll
    for (int k = 0; k < 3; ++k) { const int n = min(w + 4 * k, 9); xv[k] = ld8<0>(rx, xoff, (j * 32 + (n & 1) * 16) * (D_ * 2) + (n >> 1) * 32); }
    SB();
  };
  if (w == 0 && lane == 0) Ltk[0] = __hip_atomic_fetch_add(tk, 1u, __ATOMIC_RELAXED, __HIP_MEMORY_SCOPE_AGENT);
  BAR();
  int j = __builtin_amdgcn_readfirstlane(Ltk[0]);
  if (j >= NSEG) return;
  fetch(j, 0x30000000);
#pragma unroll 1
  for (;;) {
    STAMP(0);
    const int can = can0 + (lane < 16 ? j * 81920 : 0);
    const unsigned want = lane < 16 ? 1u : (unsigned)(j + 2);
    int left = SPIN_CAP;
    while (__builtin_amdgcn_ballot_w64(ld4<SC1>(ws, can, 0) + 1u < want) != 0ull)
      asm volatile("s_sub_u32 %0, %0, 1\n\ts_cbranch_scc0 1f\n\ts_endpgm\n1:\n\ts_sleep 2" : "+s"(left)::"scc", "memory");
    __builtin_amdgcn_fence(__ATOMIC_ACQUIRE, "wavefront");
    STAMP(1);
    unsigned nt = 0;
    if (w == 0 && lane == 0) nt = __hip_atomic_fetch_add(tk, 1u, __ATOMIC_RELAXED, __HIP_MEMORY_SCOPE_AGENT);
    const int yb = __builtin_amdgcn_readfirstlane(OFF_Y + q * 10 * ZR + j * ZS + w * 1024);
    const int pb = __builtin_amdgcn_readfirstlane(OFF_PT + j * 81920 + w * 8192), pmb = __builtin_amdgcn_readfirstlane(OFF_PT + j * 81920 + 65536 + w * 2048);
    f4 acc[5][2];
    unsigned long long bad;
    do {
      v4u y[5][2], pt[20];
#pragma unroll
      for (int k = 0; k < 16; ++k) pt[k] = ld16<SC1>(ws, lane * 16, pb + (k & 1) * 32768 + (k >> 1) * 1024);
#pragma unroll
      for (int k = 0; k < 4; ++k) pt[16 + k] = ld16<SC1>(ws, lane * 16, pmb + (k & 1) * 8192 + (k >> 1) * 1024);
#pragma unroll
      for (int t = 0; t < 5; ++t) { y[t][0] = ld16<SC1>(ws, yoff, yb + t * 2 * ZR); y[t][1] = ld16<SC1>(ws, yoff + 512, yb + t * 2 * ZR); }
      SB();
      unsigned m = 0;
#pragma unroll
      for (int t = 0; t < 5; ++t) { acc[t][0] = f4{0, 0, 0, 0}; acc[t][1] = f4{0, 0, 0, 0}; }
#pragma unroll
      for (int k = 0; k < 10; ++k) {
#pragma unroll
        for (int tt = 0; tt < 2; ++tt) {
#pragma unroll
          for (int t = 0; t < 5; ++t) acc[t][tt] = mfma32(k < 8 ? z[t][k] : y[t][k - 8], pt[2 * k + tt], acc[t][tt]);
          m = mx4(m, pt[2 * k + tt]);
        }
        SB();
      }
      bad = __builtin_amdgcn_ballot_w64(m == 0xFFFFFFFFu);
      asm volatile("s_sub_u32 %0, %0, 1\n\ts_cbranch_scc0 1f\n\ts_endpgm\n1:" : "+s"(left)::"scc", "memory");
    } while (bad != 0ull);
    STAMP(2);
    lchar* pc = L + buf * 40960 + lane * 16;
#pragma unroll
    for (int t = 0; t < 5; ++t) { lwf4(pc + w * 10240 + t * 2048, acc[t][0]); lwf4(pc + w * 10240 + t * 2048 + 1024, acc[t][1]); }
    if (w == 0 && lane == 0) Ltk[1 + buf] = nt;
    BAR();
    const int jn = __builtin_amdgcn_readfirstlane(Ltk[1 + buf]);
    STAMP(3);
#pragma unroll
    for (int k = 0; k < 3; ++k) {
      const int n = min(w + 4 * k, 9);
      const f4 x = (lrf4(pc + n * 1024) + lrf4(pc + 10240 + n * 1024)) + (lrf4(pc + 20480 + n * 1024) + lrf4(pc + 30720 + n * 1024));
      const v2u o = {pk(blo(xv[k].x) + blo(ga[k].x) * x[0], bhi(xv[k].x) + bhi(ga[k].x) * x[1]), pk(blo(xv[k].y) + blo(ga[k].y) * x[2], bhi(xv[k].y) + bhi(ga[k].y) * x[3])};
      st8<SC1>(o, ws, (g >> 1) * 512 + (lane & 15) * 16 + (g & 1) * 8 + OFF_X1 + (j * 80 + q * 10 + (n >> 1) * 2) * 512 + (n & 1) * 256, 0);
    }
    SB();
    STAMP(4);
    if (jn >= NSEG) break;
    fetch(jn, flx + j * 128);
    j = jn;
    buf ^= 1;
    STAMP(5); STAMP_FLUSH();
  }
  VMWAIT(0);
  st4<SC1>(1u, ws, flx + j * 128, 0);
}

DEVI void role_b(const Params& p, rsrc_t ws, lchar* L, int w, int hw) {
  const int lane = lane_id();
  STAMP_INIT(128 + hw, ST_MASK_O); STAMP(0);
  const v4u srd = mk_srd(p.ws, WS_BYTES);
  const unsigned lx = (unsigned)(size_t)L + w * 10240;
  v4u wq[3][20];
  {
    const int wh = hw % 3;
    const v4u sw = mk_srd(wh == 0 ? p.Wq : wh == 1 ? p.Wk : p.Wv, D_ * HE * 2);
    const int hb = (hw / 3) * (E_ * 2);
    stream_proj<5, false, HE * 2>(L, lane, w, [&](auto, auto) __attribute__((always_inline)) { return sw; },
      [&](auto gc, auto cc) __attribute__((always_inline)) { return (decltype(gc)::value * 4 + decltype(cc)::value) * 32 * (HE * 2) + hb; },
      [&](auto, auto cc) __attribute__((always_inline)) { return decltype(cc)::value; },
      [&](auto gc, auto cc, v4u (&o)[3]) __attribute__((always_inline)) {
        constexpr int ks = decltype(gc)::value * 4 + decltype(cc)::value;
        PIN_A(o[0]); PIN_A(o[1]); PIN_A(o[2]);
        wq[0][ks] = o[0]; wq[1][ks] = o[1]; wq[2][ks] = o[2];
      });
  }
  wait_flags(ws, OFF_CTRL + C_FLH + lane * 4, 1u);
  wait_flags(ws, OFF_CTRL + C_FLH + 256 + lane * 4, 1u);
  __builtin_amdgcn_fence(__ATOMIC_ACQUIRE, "agent");
  VMWAIT(0);
  BAR();
  dma_seg(srd, lx, lane * 16, OFF_HF + w * 10240);
  dma_seg(srd, lx + 40960, lane * 16, OFF_HF + 40960 + w * 10240);
  const int flb = lane == 0 ? OFF_CTRL + C_FLB + hw * 128 + w * 32 : 0x30000000;
  int so = OFF_QK + (hw / 3) * QH + (hw % 3) * 12288 + w * 3072 + lane * 16 - QS;
  VMWAIT(10);
  BAR();
  STAMP(1); STAMP_FLUSH();
  v4u xa[4][2], out[3];
#pragma unroll
  for (int k = 0; k < 2; ++k) { xa[k][0] = lr16(L + lane * 16 + k * 1024); xa[k][1] = lr16(L + lane * 16 + 20480 + k * 1024); }
#pragma unroll
  for (int k = 0; k < 3; ++k) out[k] = v4u{0, 0, 0, 0};
#pragma unroll 1
  for (int i = 0; i < NSEG; ++i) {
    STAMP(0);
    const int cur = i & 3, nxt = (i + 1) & 3, nx2 = (i + 2) & 3;
    const int xso = OFF_HF + min(i + 2, NSEG - 1) * 40960 + w * 10240, sp = i > 3 ? so : 0x30000000;
    f4 acc[3][2];
    proj_seg(acc, wq, xa, L + cur * 40960 + lane * 16, L + nxt * 40960 + lane * 16, [&](auto kc) __attribute__((always_inline)) {
      constexpr int ks = decltype(kc)::value;
      if constexpr (ks < 10) dma_1k(srd, lx + nx2 * 40960 + ks * 1024, lane * 16, xso + ks * 1024);
      else if constexpr (ks < 13) st16<SC1>(out[ks - 10], ws, sp, (ks - 10) * 1024);
      else if constexpr (ks == 17) {
        VMWAIT(13);
        __builtin_amdgcn_s_barrier();
        CFENCE();
        st4<SC1>((unsigned)i - 1u, ws, i > 4 ? flb : 0x30000000, 0);
      }
    });
    out[0] = v4u{PK4(acc[0][0]), PK4(acc[1][0])}; out[1] = v4u{PK4(acc[0][1]), PK4(acc[1][1])}; out[2] = v4u{PK4(acc[2][0]), PK4(acc[2][1])};
    so += QS;
    STAMP(1);
    if (i < 3 || i == NSEG - 1) {
#pragma unroll
      for (int k = 0; k < 3; ++k) st16<SC1>(out[k], ws, so, k * 1024);
      VMWAIT(0);
      st4<SC1>((unsigned)i + 1u, ws, flb, 0);
    }
    STAMP(2); STAMP_FLUSH();
  }
}

DEVI float gelu_t(float v) {
  constexpr float C1 = -2.8853900817779268f * 0.7978845608028654f, C2 = C1 * 0.044715f;
  return v * __builtin_amdgcn_rcpf(1.f + __builtin_amdgcn_exp2f(v * __builtin_fmaf(v * v, C2, C1)));
}
constexpr int NTEAM = 6;
DEVI void role_m1(const Params& p, rsrc_t ws, lchar* L, int w, int team, int fb) {
  const int lane = lane_id(), g = lane >> 4, tid = w * 64 + lane, row = tid & 15, part = tid >> 4;
  STAMP_INIT(256 + team * 8 + fb, 0xFF); STAMP(0);
  v4u wq[3][20];
  {
    const v4u sw = mk_srd(p.w1, D_ * F_ * 2);
    stream_proj<5, false, F_ * 2>(L, lane, w, [&](auto, auto) __attribute__((always_inline)) { return sw; },
      [&](auto gc, auto cc) __attribute__((always_inline)) { return (decltype(gc)::value * 4 + decltype(cc)::value) * 32 * (F_ * 2) + fb * 384; },
      [&](auto, auto cc) __attribute__((always_inline)) { return decltype(cc)::value; },
      [&](auto gc, auto cc, v4u (&o)[3]) __attribute__((always_inline)) {
        constexpr int ks = decltype(gc)::value * 4 + decltype(cc)::value;
        PIN_A(o[0]); PIN_A(o[1]); PIN_A(o[2]);
        wq[0][ks] = o[0]; wq[1][ks] = o[1]; wq[2][ks] = o[2];
      });
  }
  f4 cs[3], b1[3];
  {
    v4u ones = {0x3F803F80u, 0x3F803F80u, 0x3F803F80u, 0x3F803F80u};
    PIN_V(ones);
    NOPS(1);
    static_for<20>([&](auto kc) __attribute__((always_inline)) {
      constexpr int ks = decltype(kc)::value;
      f4 &c0 = cs[0], &c1 = cs[1], &c2 = cs[2];
      const v4u &w0 = wq[0][ks], &w1 = wq[1][ks], &w2 = wq[2][ks], &on = ones;
      if constexpr (ks == 0) { MF32_0B(c0, w0, on); MF32_0B(c1, w1, on); MF32_0B(c2, w2, on); } else { MF32_CB(c0, w0, on); MF32_CB(c1, w1, on); MF32_CB(c2, w2, on); }
    });
    NOPS(7);
    SB();
  }
#pragma unroll
  for (int t = 0; t < 3; ++t) { const v2u x = *(const GAS v2u*)(p.b1 + fb * 192 + (t < 2 ? 32 * w + 16 * t : 128 + 16 * w) + 4 * g); b1[t] = f4{blo(x.x), bhi(x.x), blo(x.y), bhi(x.y)}; }
  f2 ma[20], mb[20];
#pragma unroll
  for (int k = 0; k < 5; ++k) {
    const v4u sb = ld16<0>(ws, (part + 16 * k) * 16, OFF_CTRL + C_MOD + 3 * D_ * 2), cb = ld16<0>(ws, (part + 16 * k) * 16, OFF_CTRL + C_MOD + 4 * D_ * 2);
#pragma unroll
    for (int i = 0; i < 4; ++i) { ma[4 * k + i] = f2{1.f + blo(cb[i]), 1.f + bhi(cb[i])}; mb[4 * k + i] = f2{blo(sb[i]), bhi(sb[i])}; }
  }
  const int flt = lane == 0 ? OFF_CTRL + C_FLT + (team * 8 + fb) * 128 + w * 32 : 0x30000000;
  LAS f4* red = (LAS f4*)(L + 40960);
  auto stats = [&](const f2 (&v)[2][20], LAS f4* r) __attribute__((always_inline)) {
    f2 s0 = {0, 0}, q0 = {0, 0}, s1 = {0, 0}, q1 = {0, 0};
#pragma unroll
    for (int i = 0; i < 20; ++i) { s0 += v[0][i]; q0 += v[0][i] * v[0][i]; s1 += v[1][i]; q1 += v[1][i] * v[1][i]; }
    const f4 o = {xsum16_32(s0.x + s0.y), xsum16_32(q0.x + q0.y), xsum16_32(s1.x + s1.y), xsum16_32(q1.x + q1.y)};
    if (lane < 16) r[w * 16 + lane] = o;
  };
  auto finish = [&](LAS f4* r) __attribute__((always_inline)) {
    const f4 x = ((r[row] + r[16 + row]) + (r[32 + row] + r[48 + row])) * (1.f / D_);
    const float r0 = 1.f / sqrtf(fmaxf(x[1] - x[0] * x[0], 0.f) + 1e-5f), r1 = 1.f / sqrtf(fmaxf(x[3] - x[2] * x[2], 0.f) + 1e-5f);
    return f4{r0, -x[0] * r0, r1, -x[2] * r1};
  };
  unsigned fx = 0;
  STAMP(1); STAMP_FLUSH();
#pragma unroll 1
  for (int j = team; j < NSEG; j += NTEAM) {
    STAMP(0);
    const int flx = OFF_CTRL + C_FLX + j * 128 + (lane & 31) * 4;
    if (__builtin_amdgcn_ballot_w64(fx == 0u) != 0ull) {
      VMWAIT(0); st4<SC1>((unsigned)j, ws, flt, 0);
      wait_flags(ws, flx, 1u);
    }
    __builtin_amdgcn_fence(__ATOMIC_ACQUIRE, "wavefront");
    STAMP(1);
    v4u xv[2][5];
#pragma unroll
    for (int n = 0; n < 2; ++n)
#pragma unroll
      for (int k = 0; k < 5; ++k) xv[n][k] = ld16<SC1>(ws, part * 512 + row * 16 + n * 256, OFF_X1 + j * 40960 + k * 8192);
    VMWAIT(0);
    __builtin_amdgcn_s_waitcnt(0x0F70);
    st4<SC1>((unsigned)j, ws, flt, 0);
    STAMP(2);
    f2 v[2][20];
#pragma unroll
    for (int n = 0; n < 2; ++n)
#pragma unroll
      for (int i = 0; i < 20; ++i) v[n][i] = f2{blo(xv[n][i >> 2][i & 3]), bhi(xv[n][i >> 2][i & 3])};
    stats(v, red);
    BAR();
    const f4 n1 = finish(red);
#pragma unroll
    for (int n = 0; n < 2; ++n)
#pragma unroll
      for (int k = 0; k < 5; ++k) {
        v4u o;
#pragma unroll
        for (int i = 0; i < 4; ++i) {
          f2& x = v[n][4 * k + i];
          x = (x * n1[2 * n] + n1[2 * n + 1]) * ma[4 * k + i] + mb[4 * k + i];
          o[i] = pk(x.x, x.y);
        }
        lw16(L + n * 20480 + (part + 16 * k) * 256 + row * 16, o);
      }
    stats(v, red + 64);
    BAR();
    STAMP(3);
    v4u xa[4][2];
#pragma unroll
    for (int k = 0; k < 2; ++k) { xa[k][0] = lr16(L + lane * 16 + k * 1024); xa[k][1] = lr16(L + lane * 16 + 20480 + k * 1024); }
    const f4 n2 = finish(red + 64);
    f4 acc[3][2];
    proj_seg(acc, wq, xa, L + lane * 16, L + lane * 16, [&](auto kc) __attribute__((always_inline)) {
      if constexpr (decltype(kc)::value == 12) fx = ld4<SC1>(ws, flx, NTEAM * 128);
    });
    STAMP(4);
    v2u o[3][2];
#pragma unroll
    for (int t = 0; t < 3; ++t)
#pragma unroll
      for (int tt = 0; tt < 2; ++tt) {
        const f4 u = acc[t][tt] * n2[2 * tt] + (cs[t] * n2[2 * tt + 1] + b1[t]);
        o[t][tt] = v2u{pk(gelu_t(u[0]), gelu_t(u[1])), pk(gelu_t(u[2]), gelu_t(u[3]))};
      }
    PIN_V(fx);
    const int so = OFF_T + j * 98304 + fb * 12288 + w * 3072;
    st16<SC1>(v4u{o[0][0].x, o[0][0].y, o[1][0].x, o[1][0].y}, ws, lane * 16, so);
    st16<SC1>(v4u{o[0][1].x, o[0][1].y, o[1][1].x, o[1][1].y}, ws, lane * 16 + 1024, so);
    st16<SC1>(v4u{o[2][0].x, o[2][0].y, o[2][1].x, o[2][1].y}, ws, lane * 16 + 2048, so);
    STAMP(5); STAMP_FLUSH();
  }
  VMWAIT(0);
  st4<SC1>((unsigned)NSEG, ws, flt, 0);
}
DEVI void role_m2(const Params& p, rsrc_t ws, lchar* L, int w, int team, int cb) {
  const int lane = lane_id(), g = lane >> 4;
  STAMP_INIT(320 + team * 10 + cb, 0xFF); STAMP(0);
  v4u w2[4][12];
  {
    const v4u sw = mk_srd(p.w2, D_ * F_ * 2);
    const unsigned lb = (unsigned)(size_t)L + w * 24576;
    lchar* ra = L + w * 24576 + (4 * g + ((lane & 15) >> 2)) * 128 + (lane & 3) * 16;
    static_for<2>([&](auto bc) __attribute__((always_inline)) {
      constexpr int b = decltype(bc)::value;
      asm volatile("s_nop 4");
#pragma unroll
      for (int i = 0; i < 24; ++i) dma_1k(sw, lb + i * 1024, (lane >> 3) * (D_ * 2) + (lane & 7) * 16, ((2 * w + b) * 192 + i * 8) * (D_ * 2) + cb * 128);
      VMWAIT(0);
      static_for<24>([&](auto nc) __attribute__((always_inline)) {
        constexpr int ct = decltype(nc)::value / 6, ks = decltype(nc)::value % 6;
        lchar* a = ra + ks * 4096 + (ct >> 1) * 64 + (ct & 1) * 8;
        const v2u x0 = lrtr(a), x1 = lrtr(a + 2048);
        v4u x = {x0.x, x0.y, x1.x, x1.y};
        PIN_A(x);
        w2[ct][6 * b + ks] = x;
      });
      SB();
    });
  }
  BAR();
  const int pr = w >> 1, tt = w & 1, col = cb * 64 + pr * 32 + g * 8;
  const v4u b2 = *(const GAS v4u*)(p.b2 + col), gb = ld16<0>(ws, col * 2, OFF_CTRL + C_MOD + 5 * D_ * 2);
  const int flt = OFF_CTRL + C_FLT + (team * 8 + 2 * w + ((lane >> 2) & 1)) * 128 + (lane & 3) * 32;
  int buf = 0;
  STAMP(1); STAMP_FLUSH();
#pragma unroll 1
  for (int j = team; j < NSEG; j += NTEAM) {
    STAMP(0);
    wait_flags(ws, flt, (unsigned)j + 1u);
    STAMP(1);
    v4u tn[24];
#pragma unroll
    for (int k = 0; k < 24; ++k) tn[k] = ld16<SC1>(ws, lane * 16 + (k & 3) * 1024, OFF_T + j * 98304 + w * 24576 + (k >> 2) * 4096);
    const v4u xv = ld16<SC1>(ws, g * 512 + (lane & 15) * 16, OFF_X1 + j * 40960 + (cb * 8 + pr * 4) * 512 + tt * 256);
    SB();
    f4 acc[4][2];
#pragma unroll
    for (int ct = 0; ct < 4; ++ct) { acc[ct][0] = f4{0, 0, 0, 0}; acc[ct][1] = f4{0, 0, 0, 0}; }
#pragma unroll
    for (int b = 0; b < 2; ++b)
#pragma unroll
      for (int ks = 0; ks < 6; ++ks) {
        const v4u* t = tn + 12 * b;
        const int k = ks - 4;
        const v4u t0 = ks < 4 ? t[3 * ks] : v4u{t[6 * k + 2].x, t[6 * k + 2].y, t[6 * k + 5].x, t[6 * k + 5].y};
        const v4u t1 = ks < 4 ? t[3 * ks + 1] : v4u{t[6 * k + 2].z, t[6 * k + 2].w, t[6 * k + 5].z, t[6 * k + 5].w};
#pragma unroll
        for (int ct = 0; ct < 4; ++ct) { acc[ct][0] = mfma32(w2[ct][6 * b + ks], t0, acc[ct][0]); acc[ct][1] = mfma32(w2[ct][6 * b + ks], t1, acc[ct][1]); }
      }
    STAMP(2);
    lchar* pc = L + buf * 32768 + lane * 16;
#pragma unroll
    for (int ct = 0; ct < 4; ++ct) { lwf4(pc + w * 8192 + ct * 2048, acc[ct][0]); lwf4(pc + w * 8192 + ct * 2048 + 1024, acc[ct][1]); }
    BAR();
    STAMP(3);
    f4 x[2];
#pragma unroll
    for (int q = 0; q < 2; ++q) {
      lchar* a = pc + (2 * pr + q) * 2048 + tt * 1024;
      x[q] = (lrf4(a) + lrf4(a + 8192)) + (lrf4(a + 16384) + lrf4(a + 24576));
    }
    v4u o;
#pragma unroll
    for (int i = 0; i < 4; ++i)
      o[i] = pk(blo(xv[i]) + blo(gb[i]) * (x[i >> 1][2 * (i & 1)] + blo(b2[i])), bhi(xv[i]) + bhi(gb[i]) * (x[i >> 1][2 * (i & 1) + 1] + bhi(b2[i])));
    *(GAS v4u*)(p.out + (size_t)(j * 32 + tt * 16 + (lane & 15)) * D_ + col) = o;
    STAMP(4); STAMP_FLUSH();
    buf ^= 1;
  }
}

__global__ void __launch_bounds__(256, 1) k3_chain(Params p) {
  extern __shared__ __attribute__((aligned(16))) unsigned char smem[];
  lchar* L = (lchar*)smem;
  const rsrc_t ws = mkrs(p.ws, WS_BYTES);
  if (threadIdx.x == 0) {
    unsigned x = xcc_id(), slot = 0;
    for (int k = 0; k < 8; ++k) {
      const unsigned t = __hip_atomic_fetch_add((GAS unsigned*)(p.ws + OFF_CTRL + C_TICKET + x * 128), 1u, __ATOMIC_RELAXED, __HIP_MEMORY_SCOPE_AGENT);
      if (t < 32u) { slot = x * 32 + t; break; }
      x = (x + 1) & 7u;
    }
    *(LAS unsigned*)L = slot;
  }
  __syncthreads();
  const int slot = __builtin_amdgcn_readfirstlane(*(LAS unsigned*)L), w = __builtin_amdgcn_readfirstlane(threadIdx.x >> 6);
  __syncthreads();
  int dq = -1;
  if (slot < 32) role_a(p, ws, L, w, slot);
  else if (slot < 128) {
    const int x = (slot >> 5) - 1, t = slot & 31, nc = x < 2 ? 27 : 26;
    if (t < nc) role_c1(p, ws, L, w, x * 27 + t);
    else { const int d = x * 5 + t - nc; d_chores(p, ws, L, w, d); dq = d; }
  } else if ((slot & 3) != 3) role_b(p, ws, L, w, ((slot - 128) >> 2) * 3 + (slot & 3));
  else role_z(p, ws, L, w, (slot - 128) >> 2);
  if (slot >= 128) {
    VMWAIT(0);
    BAR();
    const int t = slot - 128, bi = (t >> 2) * 3 + (t & 3), m2 = (t & 3) == 3 ? 28 + (t >> 2) : bi - 68;
    if (m2 >= 0) role_m2(p, ws, L, w, m2 / 10, m2 % 10);
    else if (bi < 48) role_m1(p, ws, L, w, bi >> 3, bi & 7);
    else dq = bi - 48 + 16;
  }
  if (dq >= 0) role_d(p, ws, L, w, dq & 7, dq < 16 ? 112 + dq : 400 + dq);
}

}

extern "C" void kernel_launch(void* const* d_in, const int* in_sizes, int n_in, void* d_out, int out_size, void* d_ws, size_t ws_size,
                              hipStream_t stream) {
  Params p;
  p.x = (const u16*)d_in[0]; p.mem0 = (const u16*)d_in[1]; p.Wq = (const u16*)d_in[2]; p.Wk = (const u16*)d_in[3];
  p.Wv = (const u16*)d_in[4]; p.Wo = (const u16*)d_in[5]; p.w1 = (const u16*)d_in[8]; p.b1 = (const u16*)d_in[9];
  p.w2 = (const u16*)d_in[10]; p.b2 = (const u16*)d_in[11]; p.c = (const u16*)d_in[12]; p.wmod = (const u16*)d_in[13];
  p.out = (u16*)d_out;
  p.ws = (unsigned char*)(((uintptr_t)d_ws + 65535) & ~(uintptr_t)65535);
  (void)hipFuncSetAttribute((const void*)k0_prep, hipFuncAttributeMaxDynamicSharedMemorySize, 147456);
  (void)hipFuncSetAttribute((const void*)k3_chain, hipFuncAttributeMaxDynamicSharedMemorySize, 163840);
  k0_prep<<<256, 256, 147456, stream>>>(p);
  k3_chain<<<256, 256, 163840, stream>>>(p);
}
```

```cpp
#include <hip/hip_runtime.h>
#include <hip/hip_bf16.h>
#include <stdint.h>

namespace {
typedef unsigned short u16;
typedef unsigned v2u __attribute__((ext_vector_type(2)));
typedef unsigned v4u __attribute__((ext_vector_type(4)));
typedef unsigned v8u __attribute__((ext_vector_type(8)));
typedef float f2 __attribute__((ext_vector_type(2)));
typedef float f4 __attribute__((ext_vector_type(4)));
typedef float f16v __attribute__((ext_vector_type(16)));
typedef short s4 __attribute__((ext_vector_type(4)));
typedef __bf16 b2 __attribute__((ext_vector_type(2)));
typedef __bf16 b8 __attribute__((ext_vector_type(8)));
typedef __bf16 b16 __attribute__((ext_vector_type(16)));
#define LAS __attribute__((address_space(3)))
#define GAS __attribute__((address_space(1)))
typedef LAS unsigned char lchar;
typedef __amdgpu_buffer_rsrc_t rsrc_t;
#define DEVI __device__ __forceinline__

constexpr int S_ = 2048, D_ = 640, H_ = 32, E_ = 192, NSEG = 64, F_ = 1536, HE = H_ * E_;
constexpr int SC1 = 16;

constexpr int OFF_CTRL = 0;
constexpr int C_TICKET = 0;
constexpr int C_DTK = 1024;
constexpr int C_NEAR = 2048;
constexpr int C_FLH = 4096;
constexpr int C_FLI = 5120;
constexpr int C_FLB = 8192;
constexpr int C_FLZ = C_FLB + 96 * 128;
constexpr int C_FLY = C_FLZ + 32 * 128;
constexpr int C_FLX = C_FLY + 80 * 128;
constexpr int C_FLT = C_FLX + 64 * 128;
static_assert(C_FLT + 48 * 128 <= 49152, "ctrl");
constexpr int C_MOD = 49152;
constexpr int C_END = C_MOD + 8192;
constexpr int OFF_YR = 65536;
constexpr int YRS = 40 * 16384;
constexpr int OFF_HAND = OFF_YR + 0x200000;
constexpr int OFF_PT = OFF_HAND + NSEG * 65536;
constexpr int OFF_HF = OFF_PT + NSEG * 81920 + 0x200000;
constexpr int OFF_QK = OFF_HF + NSEG * 40960;
constexpr int QS = 3 * 12288, QH = NSEG * QS;
constexpr int OFF_Z = OFF_QK + H_ * QH;
constexpr int ZS = 20480, ZR = NSEG * ZS, OFF_Y = OFF_Z + 16384;
constexpr int OFF_X1 = OFF_Z + 80 * ZR;
constexpr int OFF_T = OFF_X1 + NSEG * 40960;
constexpr int WS_BYTES = OFF_T + NSEG * 98304;
static_assert(WS_BYTES + (6 << 20) <= 256 * 1024 * 1024, "workspace");

constexpr int SPIN_CAP = 1 << 16;
#define A_PRE 8
#define A_NAP 190
#define C_NAP 110
#define NPOIS 2304
#define Y_NAP 98
#define XZ(s) (s)
#define XQ(s) (s)
#define C1_HOLD 5
#define C1_NAP 0

DEVI float b2f(unsigned b) { return __builtin_bit_cast(float, b << 16); }
DEVI float blo(unsigned u) { return __builtin_bit_cast(float, u << 16); }
DEVI float bhi(unsigned u) { return __builtin_bit_cast(float, u & 0xFFFF0000u); }
DEVI unsigned pk(float a, float b) { f2 x = {a, b}; return __builtin_bit_cast(unsigned, __builtin_convertvector(x, b2)); }
DEVI float rb(float f) { return blo(pk(f, f)); }
DEVI unsigned nosent(unsigned u) { return min(u, 0xFFFFFFFEu); }
DEVI unsigned fu(float f) { return __builtin_bit_cast(unsigned, f); }
DEVI float uf(unsigned u) { return __builtin_bit_cast(float, u); }
DEVI f4 mfma32(v4u a, v4u b, f4 c) { return __builtin_amdgcn_mfma_f32_16x16x32_bf16(__builtin_bit_cast(b8, a), __builtin_bit_cast(b8, b), c, 0, 0, 0); }
DEVI f16v mfma3232(v4u a, v4u b, f16v c) { return __builtin_amdgcn_mfma_f32_32x32x16_bf16(__builtin_bit_cast(b8, a), __builtin_bit_cast(b8, b), c, 0, 0, 0); }
DEVI f4 mfma16(v2u a, v2u b, f4 c) { return __builtin_amdgcn_mfma_f32_16x16x16bf16_1k(__builtin_bit_cast(s4, a), __builtin_bit_cast(s4, b), c, 0, 0, 0); }
DEVI f4 smfmac(v4u a, v8u b, f4 c, int idx) { return __builtin_amdgcn_smfmac_f32_16x16x64_bf16(__builtin_bit_cast(b8, a), __builtin_bit_cast(b16, b), c, idx, 0, 0); }
DEVI v8u cat(v4u a, v4u b) { return __builtin_shufflevector(a, b, 0, 1, 2, 3, 4, 5, 6, 7); }
DEVI v4u lo4(v8u a) { return __builtin_shufflevector(a, a, 0, 1, 2, 3); }
DEVI v4u hi4(v8u a) { return __builtin_shufflevector(a, a, 4, 5, 6, 7); }
DEVI v2u lo2(v4u a) { return __builtin_shufflevector(a, a, 0, 1); }
DEVI v2u hi2(v4u a) { return __builtin_shufflevector(a, a, 2, 3); }
DEVI rsrc_t mkrs(const void* p, int bytes) { return __builtin_amdgcn_make_buffer_rsrc((void*)p, 0, bytes, 0x00020000); }
template <int AUX> DEVI v4u ld16(rsrc_t rs, int vo, int so) { return __builtin_amdgcn_raw_buffer_load_b128(rs, vo, so, AUX); }
template <int AUX> DEVI v2u ld8(rsrc_t rs, int vo, int so) { return __builtin_amdgcn_raw_buffer_load_b64(rs, vo, so, AUX); }
template <int AUX> DEVI unsigned ld4(rsrc_t rs, int vo, int so) { return __builtin_amdgcn_raw_buffer_load_b32(rs, vo, so, AUX); }
template <int AUX> DEVI void st16(v4u v, rsrc_t rs, int vo, int so) { __builtin_amdgcn_raw_buffer_store_b128(v, rs, vo + so, 0, AUX); }
template <int AUX> DEVI void st8(v2u v, rsrc_t rs, int vo, int so) { __builtin_amdgcn_raw_buffer_store_b64(v, rs, vo, so, AUX); }
template <int AUX> DEVI void st4(unsigned v, rsrc_t rs, int vo, int so) { __builtin_amdgcn_raw_buffer_store_b32(v, rs, vo, so, AUX); }
DEVI v4u lr16(lchar* p) { return *(LAS v4u*)p; }
DEVI f4 lrf4(lchar* p) { return *(LAS f4*)p; }
DEVI void lw16(lchar* p, v4u v) { *(LAS v4u*)p = v; }
DEVI void lwf4(lchar* p, f4 v) { *(LAS f4*)p = v; }
DEVI void lw8(lchar* p, v2u v) { *(LAS v2u*)p = v; }
DEVI v2u lr8(lchar* p) { return *(LAS v2u*)p; }
DEVI v2u lrtr(lchar* p) { return __builtin_bit_cast(v2u, __builtin_amdgcn_ds_read_tr16_b64_v4i16((LAS s4*)p)); }
template <int CTRL> DEVI float dppf(float x) { return uf((unsigned)__builtin_amdgcn_update_dpp(0, (int)fu(x), CTRL, 0xF, 0xF, true)); }
template <int CTRL> DEVI unsigned dppu(unsigned x) { return (unsigned)__builtin_amdgcn_update_dpp(0, (int)x, CTRL, 0xF, 0xF, true); }
DEVI unsigned mx3(unsigned a, unsigned b, unsigned c) { return max(max(a, b), c); }
DEVI unsigned mx4(unsigned m, v4u v) { return mx3(mx3(m, v.x, v.y), v.z, v.w); }
DEVI int lane_id() { return (int)__builtin_amdgcn_mbcnt_hi(~0u, __builtin_amdgcn_mbcnt_lo(~0u, 0u)); }
DEVI unsigned xcc_id() { return (unsigned)__builtin_amdgcn_s_getreg(20 | (3 << 11)) & 7u; }
#define BAR() do { asm volatile("s_waitcnt lgkmcnt(0)" ::: "memory"); __builtin_amdgcn_s_barrier(); asm volatile("" ::: "memory"); } while (0)
#define VMWAIT(n) asm volatile("s_waitcnt vmcnt(" #n ")" ::: "memory")
#define CFENCE() asm volatile("" ::: "memory")

#define STAMP(k)
#define ST_PARAM
#define ST_ARG
#define STAMP_INIT(slot, msk)
#define STAMP_FLUSH()
#define FST(k)
#define FST_DECL
#define FST_DONE()
struct Params {
  const u16 *x, *mem0, *Wq, *Wk, *Wv, *Wo, *w1, *b1, *w2, *b2, *c, *wmod;
  u16* out;
  unsigned char* ws;
};

__host__ __device__ inline int hand_shift(const void* w) { return (int)((0u - ((unsigned)(uintptr_t)w + OFF_HAND)) & 0x1FFFFFu); }
DEVI int m_line(int r) {
  const int t = r >> 3, u = r & 7;
  return t < 8 ? (t >> 1) * 16384 + 5120 + (t & 1) * 1024 + u * 128 : (2 * (t - 8) + (u >> 2)) * 16384 + 5120 + 2048 + (u & 3) * 128;
}

DEVI float xsum16_32(float v) {
  { const auto t = __builtin_amdgcn_permlane16_swap(fu(v), fu(v), false, false); v = uf(t[0]) + uf(t[1]); }
  { const auto t = __builtin_amdgcn_permlane32_swap(fu(v), fu(v), false, false); v = uf(t[0]) + uf(t[1]); }
  return v;
}
template <int N> DEVI void gather(rsrc_t ws, const int (&off)[N], int so, v4u (&v)[N]) {
  int left = SPIN_CAP;
  unsigned long long bad;
  do {
#pragma unroll
    for (int k = 0; k < N; ++k) v[k] = ld16<SC1>(ws, off[k], so);
    unsigned m = 0;
#pragma unroll
    for (int k = 0; k < N; ++k) m = mx4(m, v[k]);
    bad = __builtin_amdgcn_ballot_w64(m == 0xFFFFFFFFu);
    asm volatile("s_sub_u32 %0, %0, 1\n\ts_cbranch_scc0 1f\n\ts_endpgm\n1:" : "+s"(left)::"scc", "memory");
  } while (bad != 0ull);
}
DEVI void gather_m(rsrc_t ws, int lane, int so, v4u& a, v4u& b, v2u& c) {
  int left = SPIN_CAP;
  unsigned long long bad;
  do {
    a = ld16<SC1>(ws, lane * 16, so); b = ld16<SC1>(ws, lane * 16 + 1024, so); c = ld8<SC1>(ws, lane * 8 + 2048, so);
    bad = __builtin_amdgcn_ballot_w64(mx3(mx4(mx4(0u, a), b), c.x, c.y) == 0xFFFFFFFFu);
    asm volatile("s_sub_u32 %0, %0, 1\n\ts_cbranch_scc0 1f\n\ts_endpgm\n1:" : "+s"(left)::"scc", "memory");
  } while (bad != 0ull);
}
DEVI unsigned clk() { unsigned long long t; asm volatile("s_memrealtime %0\n\ts_waitcnt lgkmcnt(0)" : "=s"(t)::"memory"); return (unsigned)t; }
DEVI void nap_until(unsigned t) { while ((unsigned)(t - clk()) - 1u < 1000u) {} }
DEVI unsigned wait_flags(rsrc_t ws, int off, unsigned want) {
  int left = SPIN_CAP;
  unsigned f;
  for (;;) {
    f = ld4<SC1>(ws, off, 0);
    if (__builtin_amdgcn_ballot_w64(f < want) == 0ull) break;
    asm volatile("s_sub_u32 %0, %0, 1\n\ts_cbranch_scc0 1f\n\ts_endpgm\n1:\n\ts_sleep 2" : "+s"(left)::"scc", "memory");
  }
  __builtin_amdgcn_fence(__ATOMIC_ACQUIRE, "wavefront");
  return f;
}
DEVI unsigned near_mask(rsrc_t ws) {
  const unsigned long long b = __builtin_amdgcn_ballot_w64(wait_flags(ws, OFF_CTRL + C_NEAR + lane_id() * 4, 1u) == 2u);
  return (__builtin_popcountll(b & 0x1FFFFFull) > 10 ? 1u : 0u) | (__builtin_popcountll((b >> 21) & 0x1FFFFFull) > 10 ? 2u : 0u) | (__builtin_popcountll((b >> 42) & 0x1FFFFFull) > 10 ? 4u : 0u);
}
DEVI unsigned latest_of(unsigned v) {
  const unsigned now = clk();
  unsigned d = (now - v) & 0x7FFFFFFFu;
  d = min(d, dppu<0xB1>(d)); d = min(d, dppu<0x4E>(d));
  return now - __builtin_amdgcn_readfirstlane(d);
}
DEVI unsigned latest_seen(rsrc_t ws, int lane, int hb) {
  int left = SPIN_CAP;
  unsigned v;
  do {
    v = ld4<SC1>(ws, (lane & 3) * 128, hb + 7680);
    asm volatile("s_sub_u32 %0, %0, 1\n\ts_cbranch_scc0 1f\n\ts_endpgm\n1:" : "+s"(left)::"scc", "memory");
  } while (__builtin_amdgcn_ballot_w64(v == 0xFFFFFFFFu) != 0ull);
  return latest_of(v);
}
DEVI int hand_base(int hs, unsigned nm, int s) { return hs + OFF_HAND + s * 65536 + ((int)(nm >> (s >> 5)) & 1) * 8192; }

template <int V> struct IC { static constexpr int value = V; };
template <int N, int I = 0, class Fn> DEVI void static_for(Fn&& f) { if constexpr (I < N) { f(IC<I>{}); static_for<N, I + 1>(f); } }
#define SB() __builtin_amdgcn_sched_barrier(0)
#define PIN_A(x) asm volatile("" : "+a"(x))
#define PIN_V(x) asm volatile("" : "+v"(x))
#define PK4(a) pk(a[0], a[1]), pk(a[2], a[3])
#define SMF_A(acc, m, wt) asm volatile("v_smfmac_f32_16x16x64_bf16 %0, %1, %2, %3" : "+v"(acc) : "v"(m), "a"(wt), "v"(sidx))
#define SMF_V(acc, m, wt) asm volatile("v_smfmac_f32_16x16x64_bf16 %0, %1, %2, %3" : "+v"(acc) : "v"(m), "v"(wt), "v"(sidx))
#define MF32_0(d, a, b) asm volatile("v_mfma_f32_16x16x32_bf16 %0, %1, %2, 0" : "=&v"(d) : "v"(a), "v"(b))
#define MF16_C(d, a, b) asm volatile("v_mfma_f32_16x16x16_bf16 %0, %1, %2, %0" : "+v"(d) : "v"(a), "v"(b))
#define NOPS(n) asm volatile("s_nop " #n)
#define MF32_0A(d, a, b) asm volatile("v_mfma_f32_16x16x32_bf16 %0, %1, %2, 0" : "=&v"(d) : "v"(a), "a"(b))
#define MF32_CA(d, a, b) asm volatile("v_mfma_f32_16x16x32_bf16 %0, %1, %2, %0" : "+v"(d) : "v"(a), "a"(b))
#define MF32_0B(d, a, b) asm volatile("v_mfma_f32_16x16x32_bf16 %0, %1, %2, 0" : "=&v"(d) : "a"(a), "v"(b))
#define MF32_CB(d, a, b) asm volatile("v_mfma_f32_16x16x32_bf16 %0, %1, %2, %0" : "+v"(d) : "a"(a), "v"(b))
template <class FILL> DEVI void proj_seg(f4 (&acc)[3][2], const v4u (&wq)[3][20], v4u (&xa)[4][2], lchar* xb, lchar* xn, FILL fill) {
  static_for<20>([&](auto kc) __attribute__((always_inline)) {
    constexpr int ks = decltype(kc)::value;
    v4u &x0 = xa[ks & 3][0], &x1 = xa[ks & 3][1];
    f4 &a00 = acc[0][0], &a01 = acc[0][1], &a10 = acc[1][0], &a11 = acc[1][1], &a20 = acc[2][0], &a21 = acc[2][1];
    const v4u &w0 = wq[0][ks], &w1 = wq[1][ks], &w2 = wq[2][ks];
    lchar* nx = ks + 2 < 20 ? xb + (ks + 2) * 1024 : xn + (ks - 18) * 1024;
    if constexpr (ks == 0) MF32_0B(a00, w0, x0); else MF32_CB(a00, w0, x0);
    SB(); xa[(ks + 2) & 3][0] = lr16(nx); SB();
    if constexpr (ks == 0) { MF32_0B(a01, w0, x1); MF32_0B(a10, w1, x0); } else { MF32_CB(a01, w0, x1); MF32_CB(a10, w1, x0); }
    SB(); xa[(ks + 2) & 3][1] = lr16(nx + 20480); SB();
    if constexpr (ks == 0) { MF32_0B(a11, w1, x1); MF32_0B(a20, w2, x0); } else { MF32_CB(a11, w1, x1); MF32_CB(a20, w2, x0); }
    SB(); fill(kc); SB();
    if constexpr (ks == 0) MF32_0B(a21, w2, x1); else MF32_CB(a21, w2, x1);
  });
  NOPS(7);
  SB();
}
#define MF32_C(d, a, b) asm volatile("v_mfma_f32_16x16x32_bf16 %0, %1, %2, %0" : "+v"(d) : "v"(a), "v"(b))
#define TP0(d, a, b) do { if constexpr (BA) MF32_0A(d, a, b); else MF32_0(d, a, b); } while (0)
#define TPC(d, a, b) do { if constexpr (BA) MF32_CA(d, a, b); else MF32_C(d, a, b); } while (0)
template <int NT, bool BA, class PUT, class FILL> DEVI void tile_pipe(const v4u (&a0)[6], const v4u (&a1)[6], const v4u (&b)[NT][6], PUT put, FILL fill) {
  f4 c[2][2];
  v4u o;
  static_for<NT>([&](auto tc) __attribute__((always_inline)) {
    constexpr int t = decltype(tc)::value;
    f4 &c0 = c[t & 1][0], &c1 = c[t & 1][1], &p0 = c[(t + 1) & 1][0], &p1 = c[(t + 1) & 1][1];
    const v4u (&bt)[6] = b[t];
    TP0(c0, a0[0], bt[0]); TP0(c1, a1[0], bt[0]);
    SB(); fill(tc, IC<0>{}); SB();
    TPC(c0, a0[1], bt[1]); TPC(c1, a1[1], bt[1]);
    SB(); fill(tc, IC<1>{}); SB();
    TPC(c0, a0[2], bt[2]);
    SB(); if constexpr (t > 0) { o.x = pk(p0[0], p0[1]); o.y = pk(p0[2], p0[3]); } SB();
    TPC(c1, a1[2], bt[2]);
    SB(); if constexpr (t > 0) { o.z = pk(p1[0], p1[1]); o.w = pk(p1[2], p1[3]); } SB();
    TPC(c0, a0[3], bt[3]); TPC(c1, a1[3], bt[3]);
    SB(); if constexpr (t > 0) put(IC<t - 1>{}, o); SB();
    TPC(c0, a0[4], bt[4]); TPC(c1, a1[4], bt[4]);
    TPC(c0, a0[5], bt[5]); TPC(c1, a1[5], bt[5]);
  });
  NOPS(7);
  SB();
  put(IC<NT - 1>{}, v4u{PK4(c[(NT - 1) & 1][0]), PK4(c[(NT - 1) & 1][1])});
  SB();
}
DEVI float row16_max(float x) {
  asm volatile("s_nop 1\n\tv_max_f32_dpp %0, %0, %0 quad_perm:[1,0,3,2] row_mask:0xf bank_mask:0xf\n\t"
               "s_nop 1\n\tv_max_f32_dpp %0, %0, %0 quad_perm:[2,3,0,1] row_mask:0xf bank_mask:0xf\n\t"
               "s_nop 1\n\tv_max_f32_dpp %0, %0, %0 row_half_mirror row_mask:0xf bank_mask:0xf\n\t"
               "s_nop 1\n\tv_max_f32_dpp %0, %0, %0 row_mirror row_mask:0xf bank_mask:0xf" : "+v"(x));
  return x;
}
DEVI void dma_sc1(v4u srd, unsigned lds, int voff, int soff) {
  asm volatile("s_mov_b32 m0, %0\n\ts_nop 0\n\tbuffer_load_dwordx4 %1, %2, %3 offen sc1 lds" ::"s"(lds), "v"(voff), "s"(srd), "s"(soff) : "memory");
}
DEVI void dma_1k(v4u srd, unsigned lds, int voff, int soff) {
  asm volatile("s_mov_b32 m0, %0\n\ts_nop 0\n\tbuffer_load_dwordx4 %1, %2, %3 offen lds" ::"s"(lds), "v"(voff), "s"(srd), "s"(soff) : "memory");
}
DEVI v4u mk_srd(const void* p, int bytes) { const unsigned long long a = (unsigned long long)p; return v4u{(unsigned)a, (unsigned)(a >> 32) & 0xFFFFu, (unsigned)bytes, 0x00020000u}; }
template <int N> DEVI void vmwait() { asm volatile("s_waitcnt vmcnt(%0)" ::"i"(N) : "memory"); }

template <int NG, bool SPARSE, int RSB, class SRD, class SO, class PICK, class SINK, class MID> DEVI void stream_proj(lchar* L, int lane, int w, SRD srd, SO so, PICK pick, SINK sink, MID mid) {
  int vo[3];
#pragma unroll
  for (int i = 0; i < 3; ++i) {
    const int n = 64 * i + lane, row = n / 24, c = n % 24, sw = 2 * (((row >> (SPARSE ? 2 : 1)) & 1) | ((w & 1) << 1));
    vo[i] = (8 * w + row) * RSB + ((c & ~7) | ((c & 7) ^ sw)) * 16;
  }
  lchar* ra[3];
  {
    const int g = lane >> 4, q = (lane & 15) >> 2, pp = lane & 3;
    const int row = SPARSE ? 16 * (g >> 1) + 4 * (g & 1) + (q & 1) + 8 * (q >> 1) : 8 * g + q;
    const int sw = 2 * (((row >> (SPARSE ? 2 : 1)) & 1) | (((row >> 3) & 1) << 1));
#pragma unroll
    for (int t = 0; t < 3; ++t) {
      const int c = 2 * (t < 2 ? 2 * w + t : 8 + w) + (pp >> 1);
      ra[t] = L + row * 384 + ((c & ~7) | ((c & 7) ^ sw)) * 16 + (pp & 1) * 8;
    }
  }
  const unsigned lb = (unsigned)(size_t)L + w * 3072;
  auto issue = [&](auto gc) __attribute__((always_inline)) {
    constexpr int g = decltype(gc)::value;
    static_for<4>([&](auto cc) __attribute__((always_inline)) {
      constexpr int c = decltype(cc)::value;
      const int b = so(gc, cc);
#pragma unroll
      for (int i = 0; i < 3; ++i) dma_1k(srd(gc, cc), lb + ((g % 3) * 4 + c) * 12288 + i * 1024, vo[i], b);
    });
  };
  asm volatile("s_nop 4");
  static_for<3>([&](auto gc) __attribute__((always_inline)) { issue(gc); });
  mid();
  static_for<NG>([&](auto gc) __attribute__((always_inline)) {
    constexpr int g = decltype(gc)::value;
    vmwait<(NG - 1 - g < 2 ? NG - 1 - g : 2) * 12>();
    BAR();
    v4u o[4][3];
    static_for<4>([&](auto cc) __attribute__((always_inline)) {
      constexpr int c = decltype(cc)::value;
      const int off = ((g % 3) * 4 + pick(gc, cc)) * 12288;
#pragma unroll
      for (int t = 0; t < 3; ++t) { const v2u x0 = lrtr(ra[t] + off), x1 = lrtr(ra[t] + off + (SPARSE ? 2 : 4) * 384); o[c][t] = v4u{x0.x, x0.y, x1.x, x1.y}; }
    });
    BAR();
    if constexpr (g + 3 < NG) issue(IC<g + 3>{});
    static_for<4>([&](auto cc) __attribute__((always_inline)) { sink(gc, cc, o[decltype(cc)::value]); });
    SB();
  });
}

DEVI void poison_share(const Params& p, rsrc_t ws, int w, int idx) {
  const int hs = hand_shift(p.ws);
  const int tid = w * 64 + lane_id();
  const v4u poison = {0xFFFFFFFFu, 0xFFFFFFFFu, 0xFFFFFFFFu, 0xFFFFFFFFu};
  if (idx == 0) {
#pragma unroll
    for (int i = 0; i < 16; ++i) st16<SC1>(poison, ws, hs + OFF_HAND + i * 4096 + tid * 16, 0);
    VMWAIT(0);
    BAR();
    for (int n = tid; n < 640; n += 256) {
      const int r = n >> 3, i = n & 7;
      const v4u x = *(const GAS v4u*)(p.mem0 + i * D_ + 8 * r);
      const v4u v = {nosent(x.x), nosent(x.y), nosent(x.z), nosent(x.w)};
      st16<SC1>(v, ws, hs + OFF_HAND + m_line(r) + i * 16, 0);
      st16<SC1>(v, ws, hs + OFF_HAND + m_line(r) + i * 16 + 8192, 0);
    }
  } else {
#pragma unroll
    for (int i = 0; i < 11; ++i) { const int pc = 16 + (idx - 1) + 223 * i; st16<SC1>(poison, ws, pc < NPOIS ? hs + OFF_HAND + pc * 4096 + tid * 16 : 0x30000000, 0); }
  }
}
DEVI void init_done(rsrc_t ws, int w, int idx) {
  VMWAIT(0);
  BAR();
  if (w == 0) st4<SC1>(1u, ws, lane_id() == 0 ? OFF_CTRL + C_FLI + idx * 4 : 0x30000000, 0);
}
DEVI void init_wait(rsrc_t ws, int lane) {
  int left = SPIN_CAP;
  for (;;) {
    const v4u f = ld16<SC1>(ws, OFF_CTRL + C_FLI + min(lane, 55) * 16, 0);
    if (__builtin_amdgcn_ballot_w64((f.x & f.y & f.z & f.w) == 0u) == 0ull) break;
    asm volatile("s_sub_u32 %0, %0, 1\n\ts_cbranch_scc0 1f\n\ts_endpgm\n1:\n\ts_sleep 4" : "+s"(left)::"scc", "memory");
  }
  __builtin_amdgcn_fence(__ATOMIC_ACQUIRE, "wavefront");
}
DEVI void ln_stats(const f2 (&v)[2][20], LAS f4* r, int w, int lane) {
  f2 s0 = {0, 0}, q0 = {0, 0}, s1 = {0, 0}, q1 = {0, 0};
#pragma unroll
  for (int i = 0; i < 20; ++i) { s0 += v[0][i]; q0 += v[0][i] * v[0][i]; s1 += v[1][i]; q1 += v[1][i] * v[1][i]; }
  const f4 o = {xsum16_32(s0.x + s0.y), xsum16_32(q0.x + q0.y), xsum16_32(s1.x + s1.y), xsum16_32(q1.x + q1.y)};
  if (lane < 16) r[w * 16 + lane] = o;
}
DEVI f4 ln_finish(LAS f4* r, int row) {
  const f4 x = ((r[row] + r[16 + row]) + (r[32 + row] + r[48 + row])) * (1.f / D_);
  const float r0 = __builtin_amdgcn_rsqf(fmaxf(x[1] - x[0] * x[0], 0.f) + 1e-5f), r1 = __builtin_amdgcn_rsqf(fmaxf(x[3] - x[2] * x[2], 0.f) + 1e-5f);
  return f4{r0, -x[0] * r0, r1, -x[2] * r1};
}
DEVI void unpack2(const v4u (&xv)[2][5], f2 (&v)[2][20]) {
#pragma unroll
  for (int n = 0; n < 2; ++n)
#pragma unroll
    for (int i = 0; i < 20; ++i) v[n][i] = f2{blo(xv[n][i >> 2][i & 3]), bhi(xv[n][i >> 2][i & 3])};
}
DEVI void h_part1(const Params& p, rsrc_t ws, lchar* Ls, int w, int k, v4u (&xv)[2][5]) {
  const int lane = lane_id(), tid = w * 64 + lane, row = tid & 15, part = tid >> 4;
  const int cg = tid & 7, rg = tid >> 3, c = min(k, 59);
  float acc[8] = {0, 0, 0, 0, 0, 0, 0, 0};
  v4u wv[20];
  unsigned cv[10];
#pragma unroll
  for (int i = 0; i < 10; ++i) cv[i] = ((const GAS unsigned*)p.c)[rg * 10 + i];
#pragma unroll
  for (int i = 0; i < 20; ++i) wv[i] = *(const GAS v4u*)(p.wmod + (size_t)(rg * 20 + i) * (6 * D_) + c * 64 + cg * 8);
#pragma unroll
  for (int n = 0; n < 2; ++n)
#pragma unroll
    for (int i = 0; i < 5; ++i) xv[n][i] = *(const GAS v4u*)(p.x + (size_t)(k * 32 + n * 16 + row) * D_ + (part + 16 * i) * 8);
  SB();
#pragma unroll
  for (int i = 0; i < 20; ++i) {
    const float x = (i & 1) ? bhi(cv[i >> 1]) : blo(cv[i >> 1]);
    const float sv = rb(x * __builtin_amdgcn_rcpf(1.f + __expf(-x)));
#pragma unroll
    for (int j = 0; j < 4; ++j) { acc[2 * j] += sv * blo(wv[i][j]); acc[2 * j + 1] += sv * bhi(wv[i][j]); }
  }
  lwf4(Ls + (rg * 64 + cg * 8) * 4, f4{acc[0], acc[1], acc[2], acc[3]}); lwf4(Ls + (rg * 64 + cg * 8) * 4 + 16, f4{acc[4], acc[5], acc[6], acc[7]});
  SB();
  {
    f2 v[2][20];
    unpack2(xv, v);
    ln_stats(v, (LAS f4*)(Ls + 8192), w, lane);
  }
  BAR();
  if (w == 0) {
    LAS float* red = (LAS float*)Ls;
    float s = 0;
#pragma unroll
    for (int i = 0; i < 32; ++i) s += red[i * 64 + lane];
    st4<SC1>(~nosent(pk(s, dppf<0xB1>(s))), ws, (lane & 1) ? 0x30000000 : OFF_CTRL + C_MOD + (c * 64 + lane) * 2, 0);
  }
}
DEVI void h_part2(rsrc_t ws, lchar* Ls, int w, int k, const v4u (&xv)[2][5]) {
  const int lane = lane_id(), tid = w * 64 + lane, row = tid & 15, part = tid >> 4;
  const f4 n1 = ln_finish((LAS f4*)(Ls + 8192), row);
  v4u sa[5], ca[5];
  int left = SPIN_CAP;
  for (;;) {
    unsigned m = 0;
#pragma unroll
    for (int i = 0; i < 5; ++i) { sa[i] = ~ld16<SC1>(ws, (part + 16 * i) * 16, OFF_CTRL + C_MOD); ca[i] = ~ld16<SC1>(ws, (part + 16 * i) * 16, OFF_CTRL + C_MOD + D_ * 2); }
#pragma unroll
    for (int i = 0; i < 5; ++i) m = mx4(mx4(m, sa[i]), ca[i]);
    if (__builtin_amdgcn_ballot_w64(m == 0xFFFFFFFFu) == 0ull) break;
    asm volatile("s_sub_u32 %0, %0, 1\n\ts_cbranch_scc0 1f\n\ts_endpgm\n1:\n\ts_sleep 2" : "+s"(left)::"scc", "memory");
  }
#pragma unroll
  for (int n = 0; n < 2; ++n)
#pragma unroll
    for (int i = 0; i < 5; ++i) {
      v4u o;
#pragma unroll
      for (int j = 0; j < 4; ++j)
        o[j] = pk((blo(xv[n][i][j]) * n1[2 * n] + n1[2 * n + 1]) * (1.f + blo(ca[i][j])) + blo(sa[i][j]), (bhi(xv[n][i][j]) * n1[2 * n] + n1[2 * n + 1]) * (1.f + bhi(ca[i][j])) + bhi(sa[i][j]));
      st16<SC1>(o, ws, OFF_HF + k * 40960 + n * 20480 + (part + 16 * i) * 256 + row * 16, 0);
    }
  VMWAIT(0);
  BAR();
  if (w == 0) st4<SC1>(1u, ws, lane == 0 ? OFF_CTRL + C_FLH + k * 4 : 0x30000000, 0);
}

constexpr int A_W = 0, A_M = 131072, A_PM = A_M + 10240, A_PT = A_PM + 4 * 1280, A_END = A_PT + 4 * 4096;
static_assert(A_END + 1024 <= 163840, "lds");

DEVI void role_a(const Params& p, rsrc_t ws, lchar* L, int w, int h) {
  const int hs = hand_shift(p.ws);
  const int lane = lane_id(), g = lane >> 4;
  STAMP_INIT(h, ST_MASK_A); STAMP(0);
  v8u wr[44];
  lchar* Lw = L + A_W + w * 32768 + lane * 16;
  {
    const v4u sq = mk_srd(p.Wq, D_ * HE * 2), sk = mk_srd(p.Wk, D_ * HE * 2);
    v4u lo[4][3], wt[16][2];
    stream_proj<10, true, HE * 2>(L, lane, w,
      [&](auto gc, auto cc) __attribute__((always_inline)) { constexpr int gi = decltype(gc)::value, c = decltype(cc)::value; return (gi < 8 ? gi >> 2 : c >> 1) ? sk : sq; },
      [&](auto gc, auto cc) __attribute__((always_inline)) {
        constexpr int gi = decltype(gc)::value, c = decltype(cc)::value, fr = gi < 8 ? 2 * c + ((gi >> 1) & 1) : 8 + (c & 1);
        return (2 * fr + (gi & 1)) * 32 * (HE * 2) + h * (E_ * 2);
      },
      [&](auto gc, auto cc) __attribute__((always_inline)) { return decltype(gc)::value < 8 ? (decltype(cc)::value ^ w) : decltype(cc)::value; },
      [&](auto gc, auto cc, v4u (&o)[3]) __attribute__((always_inline)) {
        constexpr int gi = decltype(gc)::value, c = decltype(cc)::value;
        constexpr int j = gi < 8 ? 2 * c + ((gi >> 1) & 1) : 8 + (c & 1), q = gi < 8 ? gi >> 2 : c >> 1;
        static_for<3>([&](auto tc) __attribute__((always_inline)) {
          constexpr int t = decltype(tc)::value, n = 16 * t + 2 * (j - 2) + q;
          if constexpr (j >= 2 && n % 3 == 2) wt[n / 3][gi & 1] = o[t];
          else if constexpr ((gi & 1) == 0) lo[c][t] = o[t];
          else {
            constexpr int ri = j < 2 ? j * 6 + 2 * t + q : 12 + n - (n + 1) / 3;
            v8u x = cat(lo[c][t], o[t]);
            if constexpr (ri < 32) PIN_A(x); else PIN_V(x);
            wr[ri] = x;
          }
        });
      }, []() {});
#pragma unroll
    for (int i = 0; i < 16; ++i) { lw16(Lw + i * 2048, wt[i][0]); lw16(Lw + i * 2048 + 1024, wt[i][1]); }
  }
  const int sidx = (lane & 8) ? 0xEEEE : 0x4444;
  init_wait(ws, lane);
  const unsigned nm = near_mask(ws);
  lchar* Lm_own = L + A_M + w * 2048 + lane * 16;
  lchar* Lm_half = L + A_M + 8192 + w * 512 + lane * 8;
  lchar* Lm1 = L + A_M + (w ^ 1) * 2048 + lane * 16;
  lchar* Lm2 = L + A_M + (w ^ 2) * 2048 + lane * 16;
  lchar* Lm3 = L + A_M + (w ^ 3) * 2048 + lane * 16;
  lchar* Lm8 = L + A_M + 8192 + lane * 16;
  lchar* Lpm_w = L + A_PM + w * 1280 + (lane & 7) * 160 + 32 + g * 32 + ((lane >> 3) & 1) * 16;
  lchar* Lpm_m = g >= 2 ? L + A_PM + w * 1280 + (lane & 7) * 160 + (g - 2) * 16 + ((lane >> 3) & 1) * 8 : L + A_END + w * 256 + lane * 8;
  const int u = min(lane & 15, 9);
  lchar* Lpm_r = L + A_PM + ((w & 1) * 4 + g) * 160 + u * 16;
  constexpr int OOB = 0x30000000;
  const int pst = (w < 2 && (lane & 15) < 10 && (lane & 1) == 0) ? (h >> 3) * 16384 + (u >> 1) * 1024 + (h & 7) * 128 + ((w & 1) * 4 + g) * 16 : OOB;
  const int flb = OFF_CTRL + C_FLB + (h * 3 + (lane & 1)) * 128 + w * 32;
  const int qkoff = lane * 16, qkb = OFF_QK + h * QH + w * 3072;
  v4u sel;
  {
    const int n = lane & 15, kb = 4 * (g & 1), e = (n & 7) - kb;
    const unsigned lo = e == 0 ? 0x3F80u : e == 1 ? 0x3F800000u : 0u, hi = e == 2 ? 0x3F80u : e == 3 ? 0x3F800000u : 0u;
    sel = n < 8 ? v4u{lo, hi, 0u, 0u} : v4u{0u, 0u, lo, hi};
  }
  const int ts1 = (w & 1) * 32768 + h * 1024 + lane * 16;
  const int ts2 = lane < 16 ? 65536 + (w & 1) * 8192 + h * 256 + lane * 16 : OOB;
  lchar* Lpark = L + A_PT + w * 4096 + 1024 + (w & 1) * 2048 + lane * 16;
  lchar* Lpark2 = L + A_PT + w * 4096 + (w & 1) * 512 + (lane & 15) * 16;
  auto put_tok = [&](int s) __attribute__((always_inline)) {
    const v4u a = lr16(Lpark), b = lr16(Lpark2);
    st16<SC1>(a, ws, ts1, hs + OFF_PT + s * 81920); st16<SC1>(b, ws, ts2, hs + OFF_PT + s * 81920);
  };
  constexpr float CS = 0.07216878364870322f * 1.4426950408889634f;
  constexpr float NEG = -3.0e38f;
  const float lbias = (lane & 15) < 10 ? 0.f : NEG;

  VMWAIT(0); STAMP(1);
  wait_flags(ws, flb, 1u);
  STAMP(2); STAMP_FLUSH();
  unsigned prog = 0, tsv = 0;
  const int tsoff = (lane == 0 && h == 9 * w) ? 7680 + w * 128 : OOB;
  v4u kt[3], qt[3];
#pragma unroll
  for (int i = 0; i < 3; ++i) { kt[i] = ld16<SC1>(ws, qkoff + i * 1024, qkb + 12288); qt[i] = ld16<SC1>(ws, qkoff + i * 1024, qkb); }

#pragma unroll 1
  for (int s = 0; s < NSEG; ++s) {
    const int hb = hand_base(hs, nm, s);
    int pst_s = pst + (s + 1 < NSEG ? hb : OOB);
    f4 acc[6];
    v8u wl[4];
#pragma unroll
    for (int i = 0; i < 6; ++i) {
      f2 a, b;
      asm volatile("v_mov_b64 %0, 0\n\tv_mov_b64 %1, 0" : "=v"(a), "=v"(b));
      acc[i] = f4{a.x, a.y, b.x, b.y};
    }
#pragma unroll
    for (int i = 0; i < 4; ++i) wl[i] = cat(lr16(Lw + i * 2048), lr16(Lw + i * 2048 + 1024));
#pragma unroll
    for (int i = 0; i < 4; ++i) PIN_V(wl[i]);
    PIN_V(pst_s);
    v4u m[10];
    unsigned long long tseen;
    {
      v2u v2;
      STAMP(0);
      if (A_NAP && s > 0) nap_until(latest_of(tsv) + A_NAP);
      gather_m(ws, lane, hb + w * 16384 + 5120, m[0], m[1], v2);
      asm volatile("s_memrealtime %0" : "=s"(tseen)::"memory");
      STAMP(1);
      lw16(Lm_own, m[0]); lw16(Lm_own + 1024, m[1]); lw8(Lm_half, v2);
    }
    FST_DECL FST(0)
    SB();
#pragma unroll
    for (int n = 0; n < A_PRE; ++n) SMF_A(acc[n % 6], m[n / 6], wr[n]);
    SB();
    BAR();
    STAMP(2);
    m[2] = lr16(Lm1); m[3] = lr16(Lm1 + 1024); m[4] = lr16(Lm2); m[5] = lr16(Lm2 + 1024);
    SB();
#pragma unroll
    for (int n = A_PRE; n < 12; ++n) SMF_A(acc[n % 6], m[n / 6], wr[n]);
    SB();
    m[6] = lr16(Lm3); m[7] = lr16(Lm3 + 1024); m[8] = lr16(Lm8); m[9] = lr16(Lm8 + 1024);
    asm volatile("" : "+s"(tseen));
    st4<SC1>((unsigned)tseen & 0x7FFFFFFFu, ws, tsoff, hb);
    FST(1)
    SB();
    f4 sM, s0, s1, ft;
    v4u pa, F01;
    v2u F2;
#pragma unroll
    for (int n = 0; n < 48; ++n) {
      const int t = n / 16, i = n % 16, tl = 2 * t + (n & 1), j = 2 + i / 2, ri = 12 + n - (n + 1) / 3;
      if (n % 3 == 2) {
        SMF_V(acc[tl], m[j], wl[(n / 3) % 4]);
        SB();
        if (n / 3 + 4 < 16) wl[(n / 3) % 4] = cat(lr16(Lw + (n / 3 + 4) * 2048), lr16(Lw + (n / 3 + 4) * 2048 + 1024));
      } else if (ri < 32) SMF_A(acc[tl], m[j], wr[ri]);
      else SMF_V(acc[tl], m[j], wr[ri]);
      SB();
      if (t >= 1) {
        if (i == 4) { pa.x = pk(acc[2 * t - 2][0], acc[2 * t - 2][1]); pa.y = pk(acc[2 * t - 2][2], acc[2 * t - 2][3]); }
        if (i == 5) { pa.z = pk(acc[2 * t - 1][0], acc[2 * t - 1][1]); pa.w = pk(acc[2 * t - 1][2], acc[2 * t - 1][3]); }
        if (i == 7) MF32_0(ft, pa, sel);
        if (i == 12) { F01[2 * t - 2] = pk(ft[0], ft[1]); F01[2 * t - 1] = pk(ft[2], ft[3]); }
      }
      if (i == 15) { FST(2 + t) }
      SB();
    }
    MF32_0(s0, kt[0], F01);
    MF32_0(s1, kt[1], F01);
    SB();
    pa.x = pk(acc[4][0], acc[4][1]); pa.y = pk(acc[4][2], acc[4][3]);
    SB();
    NOPS(3);
    SB();
    pa.z = pk(acc[5][0], acc[5][1]); pa.w = pk(acc[5][2], acc[5][3]);
    SB();
    NOPS(1);
    MF32_0(ft, pa, sel);
    MF32_0(sM, F01, F01);
    NOPS(6);
    SB();
    F2 = v2u{PK4(ft)};
    SB();
    NOPS(1);
    MF16_C(s1, hi2(kt[2]), F2);
    MF16_C(s0, lo2(kt[2]), F2);
    MF16_C(sM, F2, F2);
    NOPS(6);
    FST(5)
    SB();
#pragma unroll
    for (int i = 0; i < 4; ++i) s0[i] = uf((unsigned)__builtin_amdgcn_update_dpp((int)fu(s0[i]), (int)fu(s1[i]), 0x118, 0xF, 0xC, false));
    lwf4(Lpm_w, s0);
    lw8(Lpm_m, v2u{(unsigned)__builtin_amdgcn_update_dpp((int)fu(sM[0]), (int)fu(sM[2]), 0x118, 0xF, 0xC, false),
                   (unsigned)__builtin_amdgcn_update_dpp((int)fu(sM[1]), (int)fu(sM[3]), 0x118, 0xF, 0xC, false)});
    SB();
    STAMP(3);
    BAR();
    FST(6)
    STAMP(4);
    if (w < 2) {
      const f4 y0 = lrf4(Lpm_r), y1 = lrf4(Lpm_r + 1280), y2 = lrf4(Lpm_r + 2560), y3 = lrf4(Lpm_r + 3840);
      SB();
      const f4 x = (y0 + y1) + (y2 + y3);
      const float mc = __builtin_fmaf(row16_max(fmaxf(fmaxf(x[0], x[1]), fmaxf(x[2], x[3]))), -CS, lbias);
      float e[4];
#pragma unroll
      for (int i = 0; i < 4; ++i) { e[i] = __builtin_amdgcn_exp2f(__builtin_fmaf(x[i], CS, mc)); PIN_V(e[i]); }
      float sa = e[0] + e[1], sb = e[2] + e[3];
      PIN_V(sa);
      float sum = sa + sb;
      sum += dppf<0xB1>(sum); sum += dppf<0x4E>(sum); sum += dppf<0x141>(sum); sum += dppf<0x140>(sum);
      const float rc = __builtin_amdgcn_rcpf(sum);
      const unsigned a0 = pk(__builtin_fabsf(e[0] * rc), __builtin_fabsf(e[1] * rc)), a1 = pk(__builtin_fabsf(e[2] * rc), __builtin_fabsf(e[3] * rc));
      const unsigned n0 = dppu<0xB1>(a0), n1 = dppu<0xB1>(a1);
      st16<SC1>(v4u{a0, a1, n0, n1}, ws, pst_s, 0);
    } else if (s > 0) put_tok(s - 1);
    SB();
    FST(7)
    FST_DONE();
    STAMP(5);
    SB();
    {
      f4 tm0, t00, tm1, t10, t11;
      MF32_0(tm0, F01, qt[0]); MF32_0(t00, kt[0], qt[0]); MF32_0(tm1, F01, qt[1]); MF32_0(t10, kt[0], qt[1]); MF32_0(t11, kt[1], qt[1]);
      MF16_C(tm0, F2, lo2(qt[2])); MF16_C(t00, lo2(kt[2]), lo2(qt[2])); MF16_C(tm1, F2, hi2(qt[2])); MF16_C(t10, lo2(kt[2]), hi2(qt[2])); MF16_C(t11, hi2(kt[2]), hi2(qt[2]));
      NOPS(7);
      SB();
      lchar* d = L + A_PT + w * 4096 + lane * 16;
#pragma unroll
      for (int i = 0; i < 4; ++i) tm0[i] = uf(__builtin_amdgcn_permlane32_swap(fu(tm0[i]), fu(tm1[i]), false, false)[1]);
      lwf4(d, tm0); lwf4(d + 1024, t00); lwf4(d + 2048, t10); lwf4(d + 3072, t11);
    }
    BAR();
    STAMP(6);
    if (__builtin_amdgcn_ballot_w64(prog < (unsigned)min(s + 2, NSEG)) != 0ull) wait_flags(ws, flb, (unsigned)min(s + 2, NSEG));
#pragma unroll
    for (int i = 0; i < 3; ++i) { kt[i] = ld16<SC1>(ws, qkoff + i * 1024, qkb + XQ(min(s + 1, NSEG - 1)) * QS + 12288); qt[i] = ld16<SC1>(ws, qkoff + i * 1024, qkb + XQ(min(s + 1, NSEG - 1)) * QS); }
    prog = ld4<SC1>(ws, flb, 0);
    tsv = ld4<SC1>(ws, (lane & 3) * 128, hb + 7680);
    SB();
    if (w >= 2) {
      const int a = w - 2, q = lane & 15;
      lchar* sm = L + A_PT + a * 512 + (lane & 31) * 16;
      lchar* sd = L + A_PT + 1024 + a * 2048 + lane * 16;
      f4 r[12];
#pragma unroll
      for (int i = 0; i < 4; ++i) { r[i] = lrf4(sm + i * 4096); r[4 + i] = lrf4(sd + i * 4096); }
      f4 xf = {NEG, NEG, NEG, NEG};
      if (a) xf = (lrf4(sd - 1024) + lrf4(sd + 3072)) + (lrf4(sd + 7168) + lrf4(sd + 11264));
      f4 xm = (r[0] + r[1]) + (r[2] + r[3]), xd = (r[4] + r[5]) + (r[6] + r[7]);
      float mx = NEG;
#pragma unroll
      for (int i = 0; i < 4; ++i) {
        if (4 * g + i > q) xd[i] = NEG;
        mx = fmaxf(mx, fmaxf(xm[i], fmaxf(xd[i], xf[i])));
      }
      { const auto t = __builtin_amdgcn_permlane16_swap(fu(mx), fu(mx), false, false); mx = fmaxf(uf(t[0]), uf(t[1])); }
      { const auto t = __builtin_amdgcn_permlane32_swap(fu(mx), fu(mx), false, false); mx = fmaxf(uf(t[0]), uf(t[1])); }
      const float mc = -mx * CS;
#pragma unroll
      for (int i = 0; i < 4; ++i) {
        xm[i] = __builtin_amdgcn_exp2f(__builtin_fmaf(xm[i], CS, mc));
        xd[i] = __builtin_amdgcn_exp2f(__builtin_fmaf(xd[i], CS, mc));
        xf[i] = __builtin_amdgcn_exp2f(__builtin_fmaf(xf[i], CS, mc));
      }
      const f4 tsum = xd + xf + 0.5f * xm;
      const float rc = __builtin_amdgcn_rcpf(xsum16_32((tsum[0] + tsum[1]) + (tsum[2] + tsum[3])));
#define PKA(a, b) pk(__builtin_fabsf((a) * rc), __builtin_fabsf((b) * rc))
      const v2u pd = {PKA(xd[0], xd[1]), PKA(xd[2], xd[3])}, pf = {PKA(xf[0], xf[1]), PKA(xf[2], xf[3])};
      lw16(Lpark, a ? v4u{pf.x, pf.y, pd.x, pd.y} : v4u{pd.x, pd.y, pf.x, pf.y});
      const unsigned m0 = PKA(xm[0], xm[1]), m1 = PKA(xm[2], xm[3]);
      const auto e0 = __builtin_amdgcn_permlane16_swap(m0, m0, false, false), e1 = __builtin_amdgcn_permlane16_swap(m1, m1, false, false);
      lw16(Lpark2, v4u{e0[0], e1[0], e0[1], e1[1]});
    }
    STAMP(7); STAMP_FLUSH();
  }
  if (w >= 2) put_tok(NSEG - 1);
}

DEVI void role_c1(const Params& p, rsrc_t ws, lchar* L, int w, int r, int idx) {
  const int hs = hand_shift(p.ws);
  const int lane = lane_id(), g = lane >> 4, rho = lane & 15;
  STAMP_INIT(32 + r, ST_MASK_C); STAMP(0);
  const int h = 2 * (r / 5) + (w >> 1), cg = r % 5;
  const v4u poison = {0xFFFFFFFFu, 0xFFFFFFFFu, 0xFFFFFFFFu, 0xFFFFFFFFu};
  int yown = hs + OFF_YR + (r >> 1) * 16384 + (r & 1) * 4096 + w * 1024 + lane * 16;
  v8u u[4][10];
  {
    const v4u sv = mk_srd(p.Wv, D_ * HE * 2), so_ = mk_srd(p.Wo, D_ * HE * 2);
    const unsigned l0 = (unsigned)(size_t)L;
    asm volatile("s_nop 4");
#pragma unroll
    for (int i = 0; i < 24; ++i)
      dma_1k(so_, l0 + (w * 24 + i) * 1024, g * (D_ * 2) + ((rho ^ (2 * g) ^ (((i >> 1) & 1) * 8)) * 16), (h * E_ + (w & 1) * 96 + i * 4) * (D_ * 2) + cg * 256);
    int vo[3];
#pragma unroll
    for (int i = 0; i < 3; ++i) { const int n = 64 * i + lane, row = (n * 2731) >> 16, c = n - row * 24; vo[i] = row * (HE * 2) + ((c ^ (row >> 2)) * 16 ^ ((w & 1) * 64)); }
    auto issue = [&](int n) __attribute__((always_inline)) {
      const int so = (n * 32 + (w & 1) * 16) * (HE * 2) + h * (E_ * 2);
#pragma unroll
      for (int i = 0; i < 6; ++i) dma_1k(sv, l0 + ((n + 4) % 6) * 24576 + w * 6144 + i * 1024, vo[i % 3] ^ ((i / 3) * 32), so + (i / 3) * 8 * (HE * 2));
    };
    issue(0); issue(1);
    poison_share(p, ws, w, idx);
#pragma unroll
    for (int i = 0; i < 4; ++i) st16<SC1>(poison, ws, yown, (i >> 1) * YRS + (i & 1) * 8192);
    const int srow = 16 * (rho >> 3) + 4 * ((rho >> 2) & 1) + (rho & 1) + 8 * ((rho >> 1) & 1);
    lchar* ar[2];
#pragma unroll
    for (int k = 0; k < 2; ++k) ar[k] = L + (w >> 1) * 12288 + srow * 384 + ((4 * k + g) ^ (srow >> 2)) * 16;
    VMWAIT(15);
    BAR();
    STAMP(2);
    v4u bw[4][6], lo[4];
    {
      const int q = rho >> 2, pp = lane & 3, key = 2 * (q | ((g & 1) << 2));
#pragma unroll
      for (int ct = 0; ct < 4; ++ct) {
        lchar* b = L + (w >> 1) * 49152 + (8 * g + q) * 256 + ((2 * (4 * (w & 1) + ct) + (pp >> 1)) ^ key) * 16 + (pp & 1) * 8;
#pragma unroll
        for (int ks = 0; ks < 6; ++ks) { const v2u x0 = lrtr(b + ks * 8192), x1 = lrtr(b + ks * 8192 + 1024); bw[ct][ks] = v4u{x0.x, x0.y, x1.x, x1.y}; }
      }
    }
    BAR();
    issue(2); issue(3); issue(4);
    static_for<20>([&](auto nc) __attribute__((always_inline)) {
      constexpr int n = decltype(nc)::value;
      vmwait<(19 - n < 4 ? 19 - n : 4) * 6>();
      BAR();
      v4u a0[6], a1[6];
#pragma unroll
      for (int ks = 0; ks < 6; ++ks) { lchar* x = ar[ks & 1] + ((n + 4) % 6) * 24576 + (ks >> 1) * 128; a0[ks] = lr16(x); a1[ks] = lr16(x + 2 * 384); }
      SB();
      if constexpr (n == 2) { if (w == 0) st4<SC1>(1u, ws, lane == 0 ? OFF_CTRL + C_FLI + idx * 4 : 0x30000000, 0); }
      if constexpr (n + 5 < 20) issue(n + 5);
      tile_pipe<4, false>(a0, a1, bw, [&](auto tc, v4u v) __attribute__((always_inline)) {
        constexpr int t = decltype(tc)::value;
        if constexpr ((n & 1) == 0) lo[t] = v;
        else {
          v8u x = cat(lo[t], v);
          if constexpr (n / 2 < 8) PIN_A(x); else PIN_V(x);
          u[t][n / 2] = x;
        }
      }, [&](auto, auto) __attribute__((always_inline)) {});
    });
    BAR();
  }
  constexpr int C_M = 0, C_PC = 10240, PCS = 1024 + 64;
  const int sidx = (lane & 8) ? 0xEEEE : 0x4444;
  init_wait(ws, lane);
  const unsigned nm = near_mask(ws);
  const int poff[5] = {lane * 16, 1024 + lane * 16, 2048 + lane * 16, 3072 + lane * 16, 4096 + lane * 16};
  lchar* Lm_own = L + C_M + w * 2048 + lane * 16;
  lchar* Lm_half = L + C_M + 8192 + w * 512 + lane * 8;
  lchar* Lm = L + C_M + lane * 16;
  lchar* Lpc_w = L + C_PC + w * PCS + lane * 16;
  lchar* Lpc_r = L + C_PC + (lane & 3) * PCS + ((lane >> 3) + 16 * ((lane >> 2) & 1)) * 16;
  const int mst = (lane & 7) == 0 ? m_line(r) + (lane >> 3) * 16 : 0x30000000;
  const int flz = OFF_CTRL + C_FLZ + (w * 8 + (lane & 7)) * 128 + (r / 20) * 32, fly = lane == 0 ? OFF_CTRL + C_FLY + r * 128 + w * 32 : 0x30000000;
  yown += (nm >> 2) * 8192;
  const int yst = hs + OFF_YR + (nm >> 2) * 8192 + (cg * 8 + (w & 1) * 4 + g) * 16384 + ((lane >> 3) & 1) * 4096 + h * 128 + (lane & 7) * 16;
  unsigned zprog = 0, tref = 0;
  VMWAIT(0); STAMP(1); STAMP_FLUSH();
#pragma unroll 1
  for (int s = 0; s < NSEG; ++s) {
    const int hb = hand_base(hs, nm, s);
    const bool more = s + 1 < NSEG;
    v4u yz[5];
    f4 acc[4];
#pragma unroll
    for (int i = 0; i < 4; ++i) {
      f2 a, b;
      asm volatile("v_mov_b64 %0, 0\n\tv_mov_b64 %1, 0" : "=v"(a), "=v"(b));
      acc[i] = f4{a.x, a.y, b.x, b.y};
    }
    STAMP(0);
    {
      v4u v0, v1; v2u v2;
      gather_m(ws, lane, hb + w * 16384 + 5120, v0, v1, v2);
      VMWAIT(0);
      STAMP(1);
      lw16(Lm_own, v0); lw16(Lm_own + 1024, v1); lw8(Lm_half, v2);
    }
    SB();
    st4<SC1>((unsigned)s, ws, fly, 0);
    if (__builtin_amdgcn_ballot_w64(zprog < (unsigned)s + 1u) != 0ull) zprog = wait_flags(ws, flz, (unsigned)s + 1u);
#pragma unroll
    for (int i = 0; i < 4; ++i) yz[1 + i] = ld16<SC1>(ws, i * 1024 + lane * 16, OFF_Z + r * ZR + XZ(s) * ZS + w * 4096);
    zprog = ld4<SC1>(ws, flz, 0);
    SB();
    BAR();
    v4u m[10];
#pragma unroll
    for (int t = 0; t < 10; ++t) m[t] = lr16(Lm + t * 1024);
    SB();
    static_for<10>([&](auto tc) __attribute__((always_inline)) {
      constexpr int t = decltype(tc)::value;
      f4 &c0 = acc[0], &c1 = acc[1], &c2 = acc[2], &c3 = acc[3];
      const v4u& mm = m[t];
      const v8u &u0 = u[0][t], &u1 = u[1][t], &u2 = u[2][t], &u3 = u[3][t];
      const int& sidx_ = sidx;
      {
        const int sidx = sidx_;
        if constexpr (t < 8) { SMF_A(c0, mm, u0); SMF_A(c1, mm, u1); SMF_A(c2, mm, u2); SMF_A(c3, mm, u3); }
        else { SMF_V(c0, mm, u0); SMF_V(c1, mm, u1); SMF_V(c2, mm, u2); SMF_V(c3, mm, u3); }
      }
    });
    unsigned tsv = ld4<SC1>(ws, (lane & 3) * 128, hb + 7680);
    NOPS(7);
    SB();
    {
      float X[4], Y[4];
#pragma unroll
      for (int i = 0; i < 4; ++i) {
        const auto a = __builtin_amdgcn_permlane32_swap(fu(acc[0][i]), fu(acc[2][i]), false, false);
        X[i] = uf(a[0]) + uf(a[1]);
        const auto b = __builtin_amdgcn_permlane32_swap(fu(acc[1][i]), fu(acc[3][i]), false, false);
        Y[i] = uf(b[0]) + uf(b[1]);
      }
      const auto q0 = __builtin_amdgcn_permlane16_swap(nosent(pk(X[0], X[1])), nosent(pk(Y[0], Y[1])), false, false);
      const auto q1 = __builtin_amdgcn_permlane16_swap(nosent(pk(X[2], X[3])), nosent(pk(Y[2], Y[3])), false, false);
      st16<SC1>(v4u{q0[0], q1[0], q0[1], q1[1]}, ws, yst, (s & 1) * YRS);
    }
    SB();
    STAMP(2);
    tref = __builtin_amdgcn_ballot_w64(tsv == 0xFFFFFFFFu) == 0ull ? latest_of(tsv) : latest_seen(ws, lane, hb);
    if (!more) {
      const int yo[1] = {yown};
      v4u y1[1];
      gather<1>(ws, yo, (s & 1) * YRS, y1);
      yz[0] = y1[0];
    } else {
      int mst_s = mst + hand_base(hs, nm, s + 1);
      PIN_V(mst_s);
      v4u pf[5];
      STAMP(3);
      nap_until(tref + Y_NAP);
      yz[0] = ld16<SC1>(ws, yown, (s & 1) * YRS);
      SB();
      nap_until(tref + C_NAP);
      gather<5>(ws, poff, hb + w * 16384, pf);
      if (__builtin_expect(__builtin_amdgcn_ballot_w64(mx4(0u, yz[0]) == 0xFFFFFFFFu) != 0ull, 0)) {
        const int yo[1] = {yown};
        v4u y1[1];
        gather<1>(ws, yo, (s & 1) * YRS, y1);
        yz[0] = y1[0];
        PIN_V(yz[0]);
        NOPS(1);
      }
      STAMP(4);
      f4 o;
      MF32_0(o, yz[0], pf[0]);
#pragma unroll
      for (int i = 1; i < 5; ++i) MF32_C(o, yz[i], pf[i]);
      NOPS(7);
      SB();
      lwf4(Lpc_w, o);
      BAR();
      STAMP(5);
      if (w == 0) {
        f4 x = lrf4(Lpc_r) + lrf4(Lpc_r + 40 * 16);
#pragma unroll
        for (int i = 0; i < 4; ++i) x[i] += dppf<0xB1>(x[i]);
#pragma unroll
        for (int i = 0; i < 4; ++i) x[i] += dppf<0x4E>(x[i]);
        const unsigned a0 = nosent(pk(x[0], x[1])), a1 = nosent(pk(x[2], x[3]));
        const unsigned n0 = dppu<0x104>(a0), n1 = dppu<0x104>(a1);
        st16<SC1>(v4u{a0, a1, n0, n1}, ws, mst_s, 0);
      } else __builtin_amdgcn_s_sleep(C1_HOLD);
      STAMP(6);
    }
    SB();
    st16<SC1>(poison, ws, yown, (s & 1) * YRS);
    st16<SC1>(yz[0], ws, w * 1024 + lane * 16, OFF_Y + r * ZR + s * ZS);
    nap_until(tref + A_NAP + C1_NAP);
    STAMP_FLUSH();
  }
  VMWAIT(0);
  st4<SC1>((unsigned)NSEG, ws, fly, 0);
}

DEVI void role_z(const Params& p, rsrc_t ws, lchar* L, int w, int h, int idx) {
  const int lane = lane_id(), g = lane >> 4;
  STAMP_INIT(224 + h, ST_MASK_O); STAMP(0);
  v4u wo[10][6];
  {
    const v4u so_ = mk_srd(p.Wo, D_ * HE * 2);
    int vo[10];
#pragma unroll
    for (int i = 0; i < 10; ++i) { const int n = 64 * i + lane, row = n / 80; vo[i] = (n ^ (2 * row)) * 16; }
    const int q = (lane & 15) >> 2, pp = lane & 3, row = 4 * g + q;
    const unsigned lb = (unsigned)(size_t)L + w * 10240;
    auto issue = [&](auto kc) __attribute__((always_inline)) {
      constexpr int ks = decltype(kc)::value;
#pragma unroll
      for (int i = 0; i < 10; ++i) dma_1k(so_, lb + (ks % 3) * 40960 + i * 1024, vo[i], h * E_ * D_ * 2 + ks * 40960 + w * 10240);
    };
    asm volatile("s_nop 4");
    static_for<3>([&](auto kc) __attribute__((always_inline)) { issue(kc); });
    poison_share(p, ws, w, idx);
    init_done(ws, w, idx);
    static_for<6>([&](auto kc) __attribute__((always_inline)) {
      constexpr int ks = decltype(kc)::value;
      vmwait<(5 - ks < 2 ? 5 - ks : 2) * 10>();
      BAR();
#pragma unroll
      for (int t = 0; t < 10; ++t) {
        lchar* a = L + (ks % 3) * 40960 + row * 1280 + ((w * 20 + 2 * t + (pp >> 1)) ^ (2 * (row & 7))) * 16 + (pp & 1) * 8;
        const v2u x0 = lrtr(a), x1 = lrtr(a + 16 * 1280);
        wo[t][ks] = v4u{x0.x, x0.y, x1.x, x1.y};
      }
      BAR();
      if constexpr (ks + 3 < 6) issue(IC<ks + 3>{});
      static_for<10>([&](auto tc) __attribute__((always_inline)) {
        constexpr int t = decltype(tc)::value;
        v4u x = wo[t][ks];
        PIN_A(x);
        wo[t][ks] = x;
      });
      SB();
    });
  }
  const int flv = OFF_CTRL + C_FLB + (h * 3 + 2) * 128 + (lane & 3) * 32;
  const int flz = lane == 0 ? OFF_CTRL + C_FLZ + h * 128 + w * 32 : 0x30000000;
  const int zo = OFF_Z + (w * 20 + ((lane >> 3) & 1)) * ZR + (h >> 3) * 4096 + g * 1024 + (h & 7) * 128 + (lane & 7) * 16;
  STAMP(1); STAMP_FLUSH();
  unsigned vprog = 0, fv = 0;
  bool ready = false;
  v4u vn[12];
#pragma unroll
  for (int k = 0; k < 12; ++k) vn[k] = v4u{0, 0, 0, 0};
#pragma unroll 1
  for (int i = 0; i < NSEG; ++i) {
    STAMP(0);
    if (!ready) {
      if (__builtin_amdgcn_ballot_w64(fv < (unsigned)i + 1u) != 0ull) {
        if (i > 3) { VMWAIT(0); st4<SC1>((unsigned)i, ws, flz, 0); }
        vprog = wait_flags(ws, flv, (unsigned)i + 1u);
      }
      __builtin_amdgcn_fence(__ATOMIC_ACQUIRE, "wavefront");
#pragma unroll
      for (int k = 0; k < 12; ++k) vn[k] = ld16<SC1>(ws, lane * 16, OFF_QK + h * QH + i * QS + 2 * 12288 + k * 1024);
      __builtin_amdgcn_s_waitcnt(0x0F70);
    }
    v4u va[2][6];
#pragma unroll
    for (int k = 0; k < 4; ++k) { va[0][k] = vn[3 * k]; va[1][k] = vn[3 * k + 1]; }
#pragma unroll
    for (int k = 0; k < 2; ++k) {
      va[0][4 + k] = v4u{vn[6 * k + 2].x, vn[6 * k + 2].y, vn[6 * k + 5].x, vn[6 * k + 5].y};
      va[1][4 + k] = v4u{vn[6 * k + 2].z, vn[6 * k + 2].w, vn[6 * k + 5].z, vn[6 * k + 5].w};
    }
#pragma unroll
    for (int k = 0; k < 6; ++k) { PIN_V(va[0][k]); PIN_V(va[1][k]); }
    NOPS(1);
    const int nx = i + 1 < NSEG ? i + 1 : i;
    vprog = max(vprog, fv);
    ready = __builtin_amdgcn_ballot_w64(vprog < (unsigned)nx + 1u) == 0ull;
    __builtin_amdgcn_fence(__ATOMIC_ACQUIRE, "wavefront");
    STAMP(1);
    const int vso = OFF_QK + h * QH + nx * QS + 2 * 12288, zs = zo + i * ZS;
    tile_pipe<10, true>(va[0], va[1], wo, [&](auto tc, v4u v) __attribute__((always_inline)) { st16<SC1>(v, ws, zs, decltype(tc)::value * 2 * ZR); },
      [&](auto tc, auto kc) __attribute__((always_inline)) {
        constexpr int n = 2 * decltype(tc)::value + decltype(kc)::value;
        if constexpr (n < 12) vn[n] = ld16<SC1>(ws, lane * 16, vso + n * 1024);
        if constexpr (n == 15) fv = ld4<SC1>(ws, flv, 0);
      });
    if (i < 3 || i == NSEG - 1) { VMWAIT(0); st4<SC1>((unsigned)i + 1u, ws, flz, 0); }
    else { VMWAIT(10); st4<SC1>((unsigned)i, ws, flz, 0); }
    STAMP(2); STAMP_FLUSH();
  }
}

DEVI void d_chores(const Params& p, rsrc_t ws, lchar* L, int w, int d) {
  const int hs = hand_shift(p.ws);
  const int lane = lane_id();
  {
    unsigned best[2] = {0xFFFFFFFFu, 0xFFFFFFFFu};
    const int blk = d * 4 + w;
    const int pb = hs + (blk < 42 ? OFF_HAND + (blk / 21) * 0x200000 + (blk % 21) * 65536 : OFF_YR + 2 * YRS + (blk % 11) * 65536);
#pragma unroll 1
    for (int i = 0; i < 8; ++i) {
      unsigned long long c0, c1;
      asm volatile("s_memtime %0\n\ts_waitcnt lgkmcnt(0)" : "=s"(c0)::"memory");
      st4<SC1>(0u, ws, lane == 0 ? pb + (1 + (i >> 1) % 3) * 16384 + (i & 1) * 8192 + 7936 : 0x30000000, 0);
      VMWAIT(0);
      asm volatile("s_memtime %0\n\ts_waitcnt lgkmcnt(0)" : "=s"(c1)::"memory");
      const unsigned dt = (unsigned)(c1 - c0);
      if (i & 1) best[1] = min(best[1], dt); else best[0] = min(best[0], dt);
    }
    const unsigned nearhalf = (best[1] < best[0] ? 1u : 0u) ^ (xcc_id() >> 2);
    if (lane == 0) st4<SC1>(1u + nearhalf, ws, OFF_CTRL + C_NEAR + blk * 4, 0);
  }
}
DEVI void role_d(const Params& p, rsrc_t ws, lchar* L, int w, int q, int sidx_) {
  const int hs = hand_shift(p.ws);
  const int lane = lane_id(), g = lane >> 4;
  STAMP_INIT(sidx_, 0xFF); STAMP(0); STAMP_FLUSH();
  init_wait(ws, lane);
  GAS unsigned* tk = (GAS unsigned*)(p.ws + OFF_CTRL + C_DTK + q * 128);
  LAS unsigned* Ltk = (LAS unsigned*)(L + 81920);
  const int flz = OFF_CTRL + C_FLZ + (w * 8 + ((lane >> 2) & 7)) * 128 + (lane & 3) * 32;
  const int can0 = lane < 16 ? hs + OFF_PT + 65536 + (lane & 1) * 8192 + (w * 8 + (lane >> 1)) * 256 : OFF_CTRL + C_FLY + (q * 10 + min(lane - 16, 9)) * 128 + w * 32;
  const int zoff = ((lane >> 3) & 1) * ZR + g * 1024 + (lane & 7) * 16, yoff = ((lane >> 3) & 1) * ZR + g * 128 + (lane & 7) * 16;
  const rsrc_t rx = mkrs(p.x, S_ * D_ * 2);
  const int xoff = (lane & 15) * (D_ * 2) + q * 160 + g * 8;
  v2u ga[3];
#pragma unroll
  for (int k = 0; k < 3; ++k) ga[k] = ~ld8<SC1>(ws, q * 160 + g * 8, OFF_CTRL + C_MOD + 2 * D_ * 2 + (min(w + 4 * k, 9) >> 1) * 32);
  const int flx = lane == 0 ? OFF_CTRL + C_FLX + q * 16 + w * 4 : 0x30000000;
  unsigned zprog = 0;
  int buf = 0;
  v4u z[5][8];
  v2u xv[3];
  auto fetch = [&](int j, int done) __attribute__((always_inline)) {
    if (__builtin_amdgcn_ballot_w64(zprog < (unsigned)j + 1u) != 0ull) { VMWAIT(0); st4<SC1>(1u, ws, done, 0); zprog = wait_flags(ws, flz, (unsigned)j + 1u); }
    const int zb = __builtin_amdgcn_readfirstlane(OFF_Z + q * 10 * ZR + j * ZS + w * 4096);
#pragma unroll
    for (int t = 0; t < 5; ++t) {
#pragma unroll
      for (int k = 0; k < 8; ++k) z[t][k] = ld16<SC1>(ws, zoff + k * 128, zb + t * 2 * ZR);
      if (t == 1) { SB(); VMWAIT(16); st4<SC1>(1u, ws, done, 0); SB(); }
    }
#pragma unroll
    for (int k = 0; k < 3; ++k) { const int n = min(w + 4 * k, 9); xv[k] = ld8<0>(rx, xoff, (j * 32 + (n & 1) * 16) * (D_ * 2) + (n >> 1) * 32); }
    SB();
  };
  if (w == 0 && lane == 0) Ltk[0] = __hip_atomic_fetch_add(tk, 1u, __ATOMIC_RELAXED, __HIP_MEMORY_SCOPE_AGENT);
  BAR();
  int j = __builtin_amdgcn_readfirstlane(Ltk[0]);
  if (j >= NSEG) return;
  fetch(j, 0x30000000);
#pragma unroll 1
  for (;;) {
    STAMP(0);
    const int can = can0 + (lane < 16 ? j * 81920 : 0);
    const unsigned want = lane < 16 ? 1u : (unsigned)(j + 2);
    int left = SPIN_CAP;
    while (__builtin_amdgcn_ballot_w64(ld4<SC1>(ws, can, 0) + 1u < want) != 0ull)
      asm volatile("s_sub_u32 %0, %0, 1\n\ts_cbranch_scc0 1f\n\ts_endpgm\n1:\n\ts_sleep 2" : "+s"(left)::"scc", "memory");
    __builtin_amdgcn_fence(__ATOMIC_ACQUIRE, "wavefront");
    STAMP(1);
    unsigned nt = 0;
    if (w == 0 && lane == 0) nt = __hip_atomic_fetch_add(tk, 1u, __ATOMIC_RELAXED, __HIP_MEMORY_SCOPE_AGENT);
    const int yb = __builtin_amdgcn_readfirstlane(OFF_Y + q * 10 * ZR + j * ZS + w * 1024);
    const int pb = __builtin_amdgcn_readfirstlane(hs + OFF_PT + j * 81920 + w * 8192), pmb = __builtin_amdgcn_readfirstlane(hs + OFF_PT + j * 81920 + 65536 + w * 2048);
    f4 acc[5][2];
    unsigned long long bad;
    do {
      v4u y[5][2], pt[20];
#pragma unroll
      for (int k = 0; k < 16; ++k) pt[k] = ld16<SC1>(ws, lane * 16, pb + (k & 1) * 32768 + (k >> 1) * 1024);
#pragma unroll
      for (int k = 0; k < 4; ++k) pt[16 + k] = ld16<SC1>(ws, lane * 16, pmb + (k & 1) * 8192 + (k >> 1) * 1024);
#pragma unroll
      for (int t = 0; t < 5; ++t) { y[t][0] = ld16<SC1>(ws, yoff, yb + t * 2 * ZR); y[t][1] = ld16<SC1>(ws, yoff + 512, yb + t * 2 * ZR); }
      SB();
      unsigned m = 0;
#pragma unroll
      for (int t = 0; t < 5; ++t) { acc[t][0] = f4{0, 0, 0, 0}; acc[t][1] = f4{0, 0, 0, 0}; }
#pragma unroll
      for (int k = 0; k < 10; ++k) {
#pragma unroll
        for (int tt = 0; tt < 2; ++tt) {
#pragma unroll
          for (int t = 0; t < 5; ++t) acc[t][tt] = mfma32(k < 8 ? z[t][k] : y[t][k - 8], pt[2 * k + tt], acc[t][tt]);
          m = mx4(m, pt[2 * k + tt]);
        }
        SB();
      }
      bad = __builtin_amdgcn_ballot_w64(m == 0xFFFFFFFFu);
      asm volatile("s_sub_u32 %0, %0, 1\n\ts_cbranch_scc0 1f\n\ts_endpgm\n1:" : "+s"(left)::"scc", "memory");
    } while (bad != 0ull);
    STAMP(2);
    lchar* pc = L + buf * 40960 + lane * 16;
#pragma unroll
    for (int t = 0; t < 5; ++t) { lwf4(pc + w * 10240 + t * 2048, acc[t][0]); lwf4(pc + w * 10240 + t * 2048 + 1024, acc[t][1]); }
    if (w == 0 && lane == 0) Ltk[1 + buf] = nt;
    BAR();
    const int jn = __builtin_amdgcn_readfirstlane(Ltk[1 + buf]);
    STAMP(3);
#pragma unroll
    for (int k = 0; k < 3; ++k) {
      const int n = min(w + 4 * k, 9);
      const f4 x = (lrf4(pc + n * 1024) + lrf4(pc + 10240 + n * 1024)) + (lrf4(pc + 20480 + n * 1024) + lrf4(pc + 30720 + n * 1024));
      const v2u o = {pk(blo(xv[k].x) + blo(ga[k].x) * x[0], bhi(xv[k].x) + bhi(ga[k].x) * x[1]), pk(blo(xv[k].y) + blo(ga[k].y) * x[2], bhi(xv[k].y) + bhi(ga[k].y) * x[3])};
      st8<SC1>(o, ws, (g >> 1) * 512 + (lane & 15) * 16 + (g & 1) * 8 + OFF_X1 + (j * 80 + q * 10 + (n >> 1) * 2) * 512 + (n & 1) * 256, 0);
    }
    SB();
    STAMP(4);
    if (jn >= NSEG) break;
    fetch(jn, flx + j * 128);
    j = jn;
    buf ^= 1;
    STAMP(5); STAMP_FLUSH();
  }
  VMWAIT(0);
  st4<SC1>(1u, ws, flx + j * 128, 0);
}

DEVI void role_b(const Params& p, rsrc_t ws, lchar* L, int w, int hw, int idx, int hk) {
  const int lane = lane_id();
  STAMP_INIT(128 + hw, ST_MASK_O); STAMP(0);
  const v4u srd = mk_srd(p.ws, WS_BYTES);
  const unsigned lx = (unsigned)(size_t)L + w * 10240;
  v4u wq[3][20];
  {
    const int wh = hw % 3;
    const v4u sw = mk_srd(wh == 0 ? p.Wq : wh == 1 ? p.Wk : p.Wv, D_ * HE * 2);
    const int hb = (hw / 3) * (E_ * 2);
    v4u xv[2][5];
    if (hk >= 0) h_part1(p, ws, L + 147456, w, hk, xv);
    stream_proj<5, false, HE * 2>(L, lane, w, [&](auto, auto) __attribute__((always_inline)) { return sw; },
      [&](auto gc, auto cc) __attribute__((always_inline)) { return (decltype(gc)::value * 4 + decltype(cc)::value) * 32 * (HE * 2) + hb; },
      [&](auto, auto cc) __attribute__((always_inline)) { return decltype(cc)::value; },
      [&](auto gc, auto cc, v4u (&o)[3]) __attribute__((always_inline)) {
        constexpr int ks = decltype(gc)::value * 4 + decltype(cc)::value;
        PIN_A(o[0]); PIN_A(o[1]); PIN_A(o[2]);
        wq[0][ks] = o[0]; wq[1][ks] = o[1]; wq[2][ks] = o[2];
      }, [&]() __attribute__((always_inline)) {
        poison_share(p, ws, w, idx);
        if (hk >= 0) h_part2(ws, L + 147456, w, hk, xv);
        init_done(ws, w, idx);
      });
  }
  STAMP(2);
  wait_flags(ws, OFF_CTRL + C_FLH + (lane & 15) * 4, 1u);
  STAMP(3);
  asm volatile("s_nop 4");
#pragma unroll
  for (int k = 0; k < 20; ++k) dma_sc1(srd, lx + (k / 10) * 40960 + (k % 10) * 1024, lane * 16, OFF_HF + (k / 10) * 40960 + w * 10240 + (k % 10) * 1024);
  const int flb = lane == 0 ? OFF_CTRL + C_FLB + hw * 128 + w * 32 : 0x30000000;
  int so = OFF_QK + (hw / 3) * QH + (hw % 3) * 12288 + w * 3072 + lane * 16 - QS;
  VMWAIT(10);
  BAR();
  STAMP(1); STAMP_FLUSH();
  v4u xa[4][2], out[3];
#pragma unroll
  for (int k = 0; k < 2; ++k) { xa[k][0] = lr16(L + lane * 16 + k * 1024); xa[k][1] = lr16(L + lane * 16 + 20480 + k * 1024); }
#pragma unroll
  for (int k = 0; k < 3; ++k) out[k] = v4u{0, 0, 0, 0};
#pragma unroll 1
  for (int i = 0; i < NSEG; ++i) {
    STAMP(0);
    if (i == 12) wait_flags(ws, OFF_CTRL + C_FLH + lane * 4, 1u);
    const int cur = i & 3, nxt = (i + 1) & 3, nx2 = (i + 2) & 3;
    const int xso = OFF_HF + min(i + 2, NSEG - 1) * 40960 + w * 10240, sp = i > 3 ? so : 0x30000000;
    f4 acc[3][2];
    proj_seg(acc, wq, xa, L + cur * 40960 + lane * 16, L + nxt * 40960 + lane * 16, [&](auto kc) __attribute__((always_inline)) {
      constexpr int ks = decltype(kc)::value;
      if constexpr (ks < 10) dma_sc1(srd, lx + nx2 * 40960 + ks * 1024, lane * 16, xso + ks * 1024);
      else if constexpr (ks < 13) st16<SC1>(out[ks - 10], ws, sp, (ks - 10) * 1024);
      else if constexpr (ks == 17) {
        VMWAIT(13);
        __builtin_amdgcn_s_barrier();
        CFENCE();
        st4<SC1>((unsigned)i - 1u, ws, i > 4 ? flb : 0x30000000, 0);
      }
    });
    out[0] = v4u{PK4(acc[0][0]), PK4(acc[1][0])}; out[1] = v4u{PK4(acc[0][1]), PK4(acc[1][1])}; out[2] = v4u{PK4(acc[2][0]), PK4(acc[2][1])};
    so += QS;
    STAMP(1);
    if (i < 3 || i == NSEG - 1) {
#pragma unroll
      for (int k = 0; k < 3; ++k) st16<SC1>(out[k], ws, so, k * 1024);
      VMWAIT(0);
      st4<SC1>((unsigned)i + 1u, ws, flb, 0);
    }
    STAMP(2); STAMP_FLUSH();
  }
}

DEVI float gelu_t(float v) {
  constexpr float C1 = -2.8853900817779268f * 0.7978845608028654f, C2 = C1 * 0.044715f;
  return v * __builtin_amdgcn_rcpf(1.f + __builtin_amdgcn_exp2f(v * __builtin_fmaf(v * v, C2, C1)));
}
constexpr int NTEAM = 6;
DEVI void role_m1(const Params& p, rsrc_t ws, lchar* L, int w, int team, int fb) {
  const int lane = lane_id(), g = lane >> 4, tid = w * 64 + lane, row = tid & 15, part = tid >> 4;
  STAMP_INIT(256 + team * 8 + fb, 0xFF); STAMP(0);
  v4u wq[3][20];
  {
    const v4u sw = mk_srd(p.w1, D_ * F_ * 2);
    stream_proj<5, false, F_ * 2>(L, lane, w, [&](auto, auto) __attribute__((always_inline)) { return sw; },
      [&](auto gc, auto cc) __attribute__((always_inline)) { return (decltype(gc)::value * 4 + decltype(cc)::value) * 32 * (F_ * 2) + fb * 384; },
      [&](auto, auto cc) __attribute__((always_inline)) { return decltype(cc)::value; },
      [&](auto gc, auto cc, v4u (&o)[3]) __attribute__((always_inline)) {
        constexpr int ks = decltype(gc)::value * 4 + decltype(cc)::value;
        PIN_A(o[0]); PIN_A(o[1]); PIN_A(o[2]);
        wq[0][ks] = o[0]; wq[1][ks] = o[1]; wq[2][ks] = o[2];
      }, []() {});
  }
  init_wait(ws, lane);
  f4 cs[3], b1[3];
  {
    v4u ones = {0x3F803F80u, 0x3F803F80u, 0x3F803F80u, 0x3F803F80u};
    PIN_V(ones);
    NOPS(1);
    static_for<20>([&](auto kc) __attribute__((always_inline)) {
      constexpr int ks = decltype(kc)::value;
      f4 &c0 = cs[0], &c1 = cs[1], &c2 = cs[2];
      const v4u &w0 = wq[0][ks], &w1 = wq[1][ks], &w2 = wq[2][ks], &on = ones;
      if constexpr (ks == 0) { MF32_0B(c0, w0, on); MF32_0B(c1, w1, on); MF32_0B(c2, w2, on); } else { MF32_CB(c0, w0, on); MF32_CB(c1, w1, on); MF32_CB(c2, w2, on); }
    });
    NOPS(7);
    SB();
  }
#pragma unroll
  for (int t = 0; t < 3; ++t) { const v2u x = *(const GAS v2u*)(p.b1 + fb * 192 + (t < 2 ? 32 * w + 16 * t : 128 + 16 * w) + 4 * g); b1[t] = f4{blo(x.x), bhi(x.x), blo(x.y), bhi(x.y)}; }
  f2 ma[20], mb[20];
#pragma unroll
  for (int k = 0; k < 5; ++k) {
    const v4u sb = ~ld16<SC1>(ws, (part + 16 * k) * 16, OFF_CTRL + C_MOD + 3 * D_ * 2), cb = ~ld16<SC1>(ws, (part + 16 * k) * 16, OFF_CTRL + C_MOD + 4 * D_ * 2);
#pragma unroll
    for (int i = 0; i < 4; ++i) { ma[4 * k + i] = f2{1.f + blo(cb[i]), 1.f + bhi(cb[i])}; mb[4 * k + i] = f2{blo(sb[i]), bhi(sb[i])}; }
  }
  const int flt = lane == 0 ? OFF_CTRL + C_FLT + (team * 8 + fb) * 128 + w * 32 : 0x30000000;
  LAS f4* red = (LAS f4*)(L + 40960);
  unsigned fx = 0;
  STAMP(1); STAMP_FLUSH();
#pragma unroll 1
  for (int j = team; j < NSEG; j += NTEAM) {
    STAMP(0);
    const int flx = OFF_CTRL + C_FLX + j * 128 + (lane & 31) * 4;
    if (__builtin_amdgcn_ballot_w64(fx == 0u) != 0ull) {
      VMWAIT(0); st4<SC1>((unsigned)j, ws, flt, 0);
      wait_flags(ws, flx, 1u);
    }
    __builtin_amdgcn_fence(__ATOMIC_ACQUIRE, "wavefront");
    STAMP(1);
    v4u xv[2][5];
#pragma unroll
    for (int n = 0; n < 2; ++n)
#pragma unroll
      for (int k = 0; k < 5; ++k) xv[n][k] = ld16<SC1>(ws, part * 512 + row * 16 + n * 256, OFF_X1 + j * 40960 + k * 8192);
    VMWAIT(0);
    __builtin_amdgcn_s_waitcnt(0x0F70);
    st4<SC1>((unsigned)j, ws, flt, 0);
    STAMP(2);
    f2 v[2][20];
    unpack2(xv, v);
    ln_stats(v, red, w, lane);
    BAR();
    const f4 n1 = ln_finish(red, row);
#pragma unroll
    for (int n = 0; n < 2; ++n)
#pragma unroll
      for (int k = 0; k < 5; ++k) {
        v4u o;
#pragma unroll
        for (int i = 0; i < 4; ++i) {
          f2& x = v[n][4 * k + i];
          x = (x * n1[2 * n] + n1[2 * n + 1]) * ma[4 * k + i] + mb[4 * k + i];
          o[i] = pk(x.x, x.y);
        }
        lw16(L + n * 20480 + (part + 16 * k) * 256 + row * 16, o);
      }
    ln_stats(v, red + 64, w, lane);
    BAR();
    STAMP(3);
    v4u xa[4][2];
#pragma unroll
    for (int k = 0; k < 2; ++k) { xa[k][0] = lr16(L + lane * 16 + k * 1024); xa[k][1] = lr16(L + lane * 16 + 20480 + k * 1024); }
    const f4 n2 = ln_finish(red + 64, row);
    f4 acc[3][2];
    proj_seg(acc, wq, xa, L + lane * 16, L + lane * 16, [&](auto kc) __attribute__((always_inline)) {
      if constexpr (decltype(kc)::value == 12) fx = ld4<SC1>(ws, flx, NTEAM * 128);
    });
    STAMP(4);
    v2u o[3][2];
#pragma unroll
    for (int t = 0; t < 3; ++t)
#pragma unroll
      for (int tt = 0; tt < 2; ++tt) {
        const f4 u = acc[t][tt] * n2[2 * tt] + (cs[t] * n2[2 * tt + 1] + b1[t]);
        o[t][tt] = v2u{pk(gelu_t(u[0]), gelu_t(u[1])), pk(gelu_t(u[2]), gelu_t(u[3]))};
      }
    PIN_V(fx);
    const int so = OFF_T + j * 98304 + fb * 12288 + w * 3072;
    st16<SC1>(v4u{o[0][0].x, o[0][0].y, o[1][0].x, o[1][0].y}, ws, lane * 16, so);
    st16<SC1>(v4u{o[0][1].x, o[0][1].y, o[1][1].x, o[1][1].y}, ws, lane * 16 + 1024, so);
    st16<SC1>(v4u{o[2][0].x, o[2][0].y, o[2][1].x, o[2][1].y}, ws, lane * 16 + 2048, so);
    STAMP(5); STAMP_FLUSH();
  }
  VMWAIT(0);
  st4<SC1>((unsigned)NSEG, ws, flt, 0);
}
DEVI void role_m2(const Params& p, rsrc_t ws, lchar* L, int w, int team, int cb) {
  const int lane = lane_id(), g = lane >> 4;
  STAMP_INIT(320 + team * 10 + cb, 0xFF); STAMP(0);
  v4u w2[4][12];
  {
    const v4u sw = mk_srd(p.w2, D_ * F_ * 2);
    const unsigned lb = (unsigned)(size_t)L + w * 24576;
    lchar* ra = L + w * 24576 + (4 * g + ((lane & 15) >> 2)) * 128 + (lane & 3) * 16;
    static_for<2>([&](auto bc) __attribute__((always_inline)) {
      constexpr int b = decltype(bc)::value;
      asm volatile("s_nop 4");
#pragma unroll
      for (int i = 0; i < 24; ++i) dma_1k(sw, lb + i * 1024, (lane >> 3) * (D_ * 2) + (lane & 7) * 16, ((2 * w + b) * 192 + i * 8) * (D_ * 2) + cb * 128);
      VMWAIT(0);
      static_for<24>([&](auto nc) __attribute__((always_inline)) {
        constexpr int ct = decltype(nc)::value / 6, ks = decltype(nc)::value % 6;
        lchar* a = ra + ks * 4096 + (ct >> 1) * 64 + (ct & 1) * 8;
        const v2u x0 = lrtr(a), x1 = lrtr(a + 2048);
        v4u x = {x0.x, x0.y, x1.x, x1.y};
        PIN_A(x);
        w2[ct][6 * b + ks] = x;
      });
      SB();
    });
  }
  BAR();
  init_wait(ws, lane);
  const int pr = w >> 1, tt = w & 1, col = cb * 64 + pr * 32 + g * 8;
  const v4u b2 = *(const GAS v4u*)(p.b2 + col), gb = ~ld16<SC1>(ws, col * 2, OFF_CTRL + C_MOD + 5 * D_ * 2);
  const int flt = OFF_CTRL + C_FLT + (team * 8 + 2 * w + ((lane >> 2) & 1)) * 128 + (lane & 3) * 32;
  int buf = 0;
  STAMP(1); STAMP_FLUSH();
#pragma unroll 1
  for (int j = team; j < NSEG; j += NTEAM) {
    STAMP(0);
    wait_flags(ws, flt, (unsigned)j + 1u);
    STAMP(1);
    v4u tn[24];
#pragma unroll
    for (int k = 0; k < 24; ++k) tn[k] = ld16<SC1>(ws, lane * 16 + (k & 3) * 1024, OFF_T + j * 98304 + w * 24576 + (k >> 2) * 4096);
    const v4u xv = ld16<SC1>(ws, g * 512 + (lane & 15) * 16, OFF_X1 + j * 40960 + (cb * 8 + pr * 4) * 512 + tt * 256);
    SB();
    f4 acc[4][2];
#pragma unroll
    for (int ct = 0; ct < 4; ++ct) { acc[ct][0] = f4{0, 0, 0, 0}; acc[ct][1] = f4{0, 0, 0, 0}; }
#pragma unroll
    for (int b = 0; b < 2; ++b)
#pragma unroll
      for (int ks = 0; ks < 6; ++ks) {
        const v4u* t = tn + 12 * b;
        const int k = ks - 4;
        const v4u t0 = ks < 4 ? t[3 * ks] : v4u{t[6 * k + 2].x, t[6 * k + 2].y, t[6 * k + 5].x, t[6 * k + 5].y};
        const v4u t1 = ks < 4 ? t[3 * ks + 1] : v4u{t[6 * k + 2].z, t[6 * k + 2].w, t[6 * k + 5].z, t[6 * k + 5].w};
#pragma unroll
        for (int ct = 0; ct < 4; ++ct) { acc[ct][0] = mfma32(w2[ct][6 * b + ks], t0, acc[ct][0]); acc[ct][1] = mfma32(w2[ct][6 * b + ks], t1, acc[ct][1]); }
      }
    STAMP(2);
    lchar* pc = L + buf * 32768 + lane * 16;
#pragma unroll
    for (int ct = 0; ct < 4; ++ct) { lwf4(pc + w * 8192 + ct * 2048, acc[ct][0]); lwf4(pc + w * 8192 + ct * 2048 + 1024, acc[ct][1]); }
    BAR();
    STAMP(3);
    f4 x[2];
#pragma unroll
    for (int q = 0; q < 2; ++q) {
      lchar* a = pc + (2 * pr + q) * 2048 + tt * 1024;
      x[q] = (lrf4(a) + lrf4(a + 8192)) + (lrf4(a + 16384) + lrf4(a + 24576));
    }
    v4u o;
#pragma unroll
    for (int i = 0; i < 4; ++i)
      o[i] = pk(blo(xv[i]) + blo(gb[i]) * (x[i >> 1][2 * (i & 1)] + blo(b2[i])), bhi(xv[i]) + bhi(gb[i]) * (x[i >> 1][2 * (i & 1) + 1] + bhi(b2[i])));
    *(GAS v4u*)(p.out + (size_t)(j * 32 + tt * 16 + (lane & 15)) * D_ + col) = o;
    STAMP(4); STAMP_FLUSH();
    buf ^= 1;
  }
}

__global__ void __launch_bounds__(256, 1) k3_chain(Params p) {
  extern __shared__ __attribute__((aligned(16))) unsigned char smem[];
  lchar* L = (lchar*)smem;
  const rsrc_t ws = mkrs(p.ws, WS_BYTES);
  if (threadIdx.x >= 128) {
    const unsigned t = ld4<0>(ws, (threadIdx.x - 128) * 0x200000 + 61440, 0);
    asm volatile("" ::"v"(t));
  }
  if (threadIdx.x == 0) {
    unsigned x = xcc_id(), slot = 0;
    for (int k = 0; k < 8; ++k) {
      const unsigned t = __hip_atomic_fetch_add((GAS unsigned*)(p.ws + OFF_CTRL + C_TICKET + x * 128), 1u, __ATOMIC_RELAXED, __HIP_MEMORY_SCOPE_AGENT);
      if (t < 32u) { slot = x * 32 + t; break; }
      x = (x + 1) & 7u;
    }
    *(LAS unsigned*)L = slot;
  }
  __syncthreads();
  const int slot = __builtin_amdgcn_readfirstlane(*(LAS unsigned*)L), w = __builtin_amdgcn_readfirstlane(threadIdx.x >> 6);
  __syncthreads();
  const int x = slot >> 5, t = slot & 31;
  const int nc = x < 3 ? 27 : 26, ai = x == 0 ? t : -1, ci = (x - 1) * 27 + t, idx = slot - 32;
  const int bi = __builtin_amdgcn_readfirstlane(x == 0 ? -1 : x < 4 ? (t < nc ? -1 : (x - 1) * 5 + t - nc) : t < 20 ? 16 + (x - 4) * 20 + t : -1);
  const int zi = __builtin_amdgcn_readfirstlane(x >= 4 && t >= 20 && t < 28 ? (x - 4) * 8 + t - 20 : -1);
  const int dq = __builtin_amdgcn_readfirstlane(x >= 4 && t >= 28 ? (x - 4) * 4 + t - 28 : (bi >= 48 && bi < 68) ? bi - 32 : -1);
  const int m2 = __builtin_amdgcn_readfirstlane(zi >= 0 ? 28 + zi : bi - 68);
  const int qk = x < 4 ? bi : 16 + (x - 4) * 12 + t - 8;
  const int hw = __builtin_amdgcn_readfirstlane(x >= 4 && t < 8 ? ((x - 4) * 8 + t) * 3 + 2 : (qk >> 1) * 3 + (qk & 1));
  const int hk = __builtin_amdgcn_readfirstlane(x < 4 ? -1 : t >= 28 ? 48 + dq : (t >= 8 && t < 20) ? (t - 8) * 4 + x - 4 : -1);
  if (ai >= 0) role_a(p, ws, L, w, ai);
  else if (bi >= 0) role_b(p, ws, L, w, hw, idx, hk);
  else if (zi >= 0) role_z(p, ws, L, w, zi, idx);
  else if (dq >= 0) {
    v4u xv[2][5];
    h_part1(p, ws, L, w, hk, xv);
    poison_share(p, ws, w, idx);
    h_part2(ws, L, w, hk, xv);
    init_done(ws, w, idx);
    d_chores(p, ws, L, w, dq);
  }
  else role_c1(p, ws, L, w, ci, idx);
  if (bi >= 0 || zi >= 0) {
    VMWAIT(0);
    BAR();
    if (m2 >= 0) role_m2(p, ws, L, w, m2 / 10, m2 % 10);
    else if (bi < 48) role_m1(p, ws, L, w, bi >> 3, bi & 7);
  }
  if (dq >= 0) role_d(p, ws, L, w, dq & 7, dq < 16 ? 112 + dq : 400 + dq);
}

}

extern "C" void kernel_launch(void* const* d_in, const int* in_sizes, int n_in, void* d_out, int out_size, void* d_ws, size_t ws_size,
                              hipStream_t stream) {
  Params p;
  p.x = (const u16*)d_in[0]; p.mem0 = (const u16*)d_in[1]; p.Wq = (const u16*)d_in[2]; p.Wk = (const u16*)d_in[3];
  p.Wv = (const u16*)d_in[4]; p.Wo = (const u16*)d_in[5]; p.w1 = (const u16*)d_in[8]; p.b1 = (const u16*)d_in[9];
  p.w2 = (const u16*)d_in[10]; p.b2 = (const u16*)d_in[11]; p.c = (const u16*)d_in[12]; p.wmod = (const u16*)d_in[13];
  p.out = (u16*)d_out;
  p.ws = (unsigned char*)d_ws;
  (void)hipFuncSetAttribute((const void*)k3_chain, hipFuncAttributeMaxDynamicSharedMemorySize, 163840);
  (void)hipMemsetAsync((unsigned char*)d_ws + OFF_CTRL, 0, C_END, stream);
  k3_chain<<<256, 256, 163840, stream>>>(p);
}
```

```cpp
#include <hip/hip_runtime.h>
#include <hip/hip_bf16.h>
#include <stdint.h>

namespace {
typedef unsigned short u16;
typedef unsigned v2u __attribute__((ext_vector_type(2)));
typedef unsigned v4u __attribute__((ext_vector_type(4)));
typedef unsigned v8u __attribute__((ext_vector_type(8)));
typedef float f2 __attribute__((ext_vector_type(2)));
typedef float f4 __attribute__((ext_vector_type(4)));
typedef float f16v __attribute__((ext_vector_type(16)));
typedef short s4 __attribute__((ext_vector_type(4)));
typedef __bf16 b2 __attribute__((ext_vector_type(2)));
typedef __bf16 b8 __attribute__((ext_vector_type(8)));
typedef __bf16 b16 __attribute__((ext_vector_type(16)));
#define LAS __attribute__((address_space(3)))
#define GAS __attribute__((address_space(1)))
typedef LAS unsigned char lchar;
typedef __amdgpu_buffer_rsrc_t rsrc_t;
#define DEVI __device__ __forceinline__

constexpr int S_ = 2048, D_ = 640, H_ = 32, E_ = 192, NSEG = 64, F_ = 1536, HE = H_ * E_;
constexpr int SC1 = 16;

constexpr int OFF_CTRL = 0;
constexpr int C_TICKET = 0;
constexpr int C_DTK = 1024;
constexpr int C_FLP = 2048;
constexpr int C_FLH = 4096;
constexpr int C_FLB = 8192;
constexpr int C_FLZ = C_FLB + 96 * 128;
constexpr int C_FLY = C_FLZ + 32 * 128;
constexpr int C_FLX = C_FLY + 80 * 128;
constexpr int C_FLT = C_FLX + 64 * 128;
static_assert(C_FLT + 48 * 128 <= 49152, "ctrl");
constexpr int C_MOD = 49152;
constexpr int C_NEAR = C_MOD + 8192;
constexpr int OFF_YR = 65536;
constexpr int YRS = 40 * 16384;
constexpr int OFF_HAND = OFF_YR + 0x200000;
constexpr int OFF_PT = OFF_HAND + NSEG * 65536;
constexpr int OFF_HF = OFF_PT + NSEG * 81920 + 0x200000;
constexpr int OFF_QK = OFF_HF + NSEG * 40960;
constexpr int QS = 3 * 12288, QH = NSEG * QS;
constexpr int OFF_Z = OFF_QK + H_ * QH;
constexpr int ZR = 8 * 20480, OFF_Y = OFF_Z + 16384;
__host__ __device__ constexpr int zseg(int s) { return (s >> 3) * (80 * ZR) + (s & 7) * 20480; }
constexpr int OFF_X1 = OFF_Z + 8 * 80 * ZR;
constexpr int OFF_T = OFF_X1 + NSEG * 40960;
constexpr int WS_BYTES = OFF_T + NSEG * 98304;
static_assert(WS_BYTES + (6 << 20) <= 256 * 1024 * 1024, "workspace");

constexpr int SPIN_CAP = 1 << 20;
constexpr int LATE_ = 61;
constexpr int LATE = LATE_;
constexpr int A_OFF = 38;
constexpr int C_NAP = 15;
constexpr int Y_NAP = 8;
constexpr int P_AUX = 16;
constexpr int NPROBE = 6;
constexpr int B_PACE = 160;
constexpr int B_FREE = 8;
constexpr int SLOW_ADD = 10;
constexpr int B_FLAG = 11;
constexpr int QK_DELAY = 300;
constexpr int B_STAG = 0;
constexpr int M_GAP = 12;
constexpr int T_NAP = 105;
constexpr int D_STAG = 20;
constexpr int D_MAX = 36;
constexpr int CALM_FAR = 48;
constexpr int CALM_NEAR = 8;
constexpr int DUR0 = 92;
constexpr int NPOI = 6;

DEVI float b2f(unsigned b) { return __builtin_bit_cast(float, b << 16); }
DEVI float blo(unsigned u) { return __builtin_bit_cast(float, u << 16); }
DEVI float bhi(unsigned u) { return __builtin_bit_cast(float, u & 0xFFFF0000u); }
DEVI unsigned pk(float a, float b) { f2 x = {a, b}; return __builtin_bit_cast(unsigned, __builtin_convertvector(x, b2)); }
DEVI float rb(float f) { return blo(pk(f, f)); }
DEVI unsigned nosent(unsigned u) { return min(u, 0xFFFFFFFEu); }
DEVI unsigned fu(float f) { return __builtin_bit_cast(unsigned, f); }
DEVI float uf(unsigned u) { return __builtin_bit_cast(float, u); }
DEVI f4 mfma32(v4u a, v4u b, f4 c) { return __builtin_amdgcn_mfma_f32_16x16x32_bf16(__builtin_bit_cast(b8, a), __builtin_bit_cast(b8, b), c, 0, 0, 0); }
DEVI f16v mfma3232(v4u a, v4u b, f16v c) { return __builtin_amdgcn_mfma_f32_32x32x16_bf16(__builtin_bit_cast(b8, a), __builtin_bit_cast(b8, b), c, 0, 0, 0); }
DEVI f4 mfma16(v2u a, v2u b, f4 c) { return __builtin_amdgcn_mfma_f32_16x16x16bf16_1k(__builtin_bit_cast(s4, a), __builtin_bit_cast(s4, b), c, 0, 0, 0); }
DEVI f4 smfmac(v4u a, v8u b, f4 c, int idx) { return __builtin_amdgcn_smfmac_f32_16x16x64_bf16(__builtin_bit_cast(b8, a), __builtin_bit_cast(b16, b), c, idx, 0, 0); }
DEVI v8u cat(v4u a, v4u b) { return __builtin_shufflevector(a, b, 0, 1, 2, 3, 4, 5, 6, 7); }
DEVI v4u lo4(v8u a) { return __builtin_shufflevector(a, a, 0, 1, 2, 3); }
DEVI v4u hi4(v8u a) { return __builtin_shufflevector(a, a, 4, 5, 6, 7); }
DEVI v2u lo2(v4u a) { return __builtin_shufflevector(a, a, 0, 1); }
DEVI v2u hi2(v4u a) { return __builtin_shufflevector(a, a, 2, 3); }
DEVI rsrc_t mkrs(const void* p, int bytes) { return __builtin_amdgcn_make_buffer_rsrc((void*)p, 0, bytes, 0x00020000); }
template <int AUX> DEVI v4u ld16(rsrc_t rs, int vo, int so) { return __builtin_amdgcn_raw_buffer_load_b128(rs, vo, so, AUX); }
template <int AUX> DEVI v2u ld8(rsrc_t rs, int vo, int so) { return __builtin_amdgcn_raw_buffer_load_b64(rs, vo, so, AUX); }
template <int AUX> DEVI unsigned ld4(rsrc_t rs, int vo, int so) { return __builtin_amdgcn_raw_buffer_load_b32(rs, vo, so, AUX); }
template <int AUX> DEVI void st16(v4u v, rsrc_t rs, int vo, int so) { __builtin_amdgcn_raw_buffer_store_b128(v, rs, vo + so, 0, AUX); }
template <int AUX> DEVI void st8(v2u v, rsrc_t rs, int vo, int so) { __builtin_amdgcn_raw_buffer_store_b64(v, rs, vo, so, AUX); }
template <int AUX> DEVI void st4(unsigned v, rsrc_t rs, int vo, int so) { __builtin_amdgcn_raw_buffer_store_b32(v, rs, vo, so, AUX); }
DEVI v4u lr16(lchar* p) { return *(LAS v4u*)p; }
DEVI f4 lrf4(lchar* p) { return *(LAS f4*)p; }
DEVI void lw16(lchar* p, v4u v) { *(LAS v4u*)p = v; }
DEVI void lwf4(lchar* p, f4 v) { *(LAS f4*)p = v; }
DEVI void lw8(lchar* p, v2u v) { *(LAS v2u*)p = v; }
DEVI v2u lr8(lchar* p) { return *(LAS v2u*)p; }
DEVI v2u lrtr(lchar* p) { return __builtin_bit_cast(v2u, __builtin_amdgcn_ds_read_tr16_b64_v4i16((LAS s4*)p)); }
template <int CTRL> DEVI float dppf(float x) { return uf((unsigned)__builtin_amdgcn_update_dpp(0, (int)fu(x), CTRL, 0xF, 0xF, true)); }
template <int CTRL> DEVI unsigned dppu(unsigned x) { return (unsigned)__builtin_amdgcn_update_dpp(0, (int)x, CTRL, 0xF, 0xF, true); }
DEVI unsigned mx3(unsigned a, unsigned b, unsigned c) { return max(max(a, b), c); }
DEVI unsigned mx4(unsigned m, v4u v) { return mx3(mx3(m, v.x, v.y), v.z, v.w); }
DEVI int lane_id() { return (int)__builtin_amdgcn_mbcnt_hi(~0u, __builtin_amdgcn_mbcnt_lo(~0u, 0u)); }
DEVI unsigned xcc_id() { return (unsigned)__builtin_amdgcn_s_getreg(20 | (3 << 11)) & 7u; }
#define BAR() do { asm volatile("s_waitcnt lgkmcnt(0)" ::: "memory"); __builtin_amdgcn_s_barrier(); asm volatile("" ::: "memory"); } while (0)
#define VMWAIT(n) asm volatile("s_waitcnt vmcnt(" #n ")" ::: "memory")
#define CFENCE() asm volatile("" ::: "memory")

struct Params {
  const u16 *x, *mem0, *Wq, *Wk, *Wv, *Wo, *w1, *b1, *w2, *b2, *c, *wmod;
  u16* out;
  unsigned char* ws;
};

__host__ __device__ inline int hand_shift(const void* w) { return (int)((0u - ((unsigned)(uintptr_t)w + OFF_HAND)) & 0x1FFFFFu); }
DEVI int m_line(int r) {
  const int t = r >> 3, u = r & 7;
  return t < 8 ? (t >> 1) * 16384 + 5120 + (t & 1) * 1024 + u * 128 : (2 * (t - 8) + (u >> 2)) * 16384 + 5120 + 2048 + (u & 3) * 128;
}

DEVI float xsum16_32(float v) {
  { const auto t = __builtin_amdgcn_permlane16_swap(fu(v), fu(v), false, false); v = uf(t[0]) + uf(t[1]); }
  { const auto t = __builtin_amdgcn_permlane32_swap(fu(v), fu(v), false, false); v = uf(t[0]) + uf(t[1]); }
  return v;
}
template <int N> DEVI void gather(rsrc_t ws, const int (&off)[N], int so, v4u (&v)[N]) {
  int left = SPIN_CAP;
  unsigned long long bad;
  do {
#pragma unroll
    for (int k = 0; k < N; ++k) v[k] = ld16<SC1>(ws, off[k], so);
    unsigned m = 0;
#pragma unroll
    for (int k = 0; k < N; ++k) m = mx4(m, v[k]);
    bad = __builtin_amdgcn_ballot_w64(m == 0xFFFFFFFFu);
    asm volatile("s_sub_u32 %0, %0, 1\n\ts_cbranch_scc0 1f\n\ts_endpgm\n1:" : "+s"(left)::"scc", "memory");
  } while (bad != 0ull);
}
DEVI void gather_m(rsrc_t ws, int lane, int so, v4u& a, v4u& b, v2u& c) {
  int left = SPIN_CAP;
  unsigned long long bad;
  do {
    a = ld16<SC1>(ws, lane * 16, so); b = ld16<SC1>(ws, lane * 16 + 1024, so); c = ld8<SC1>(ws, lane * 8 + 2048, so);
    bad = __builtin_amdgcn_ballot_w64(mx3(mx4(mx4(0u, a), b), c.x, c.y) == 0xFFFFFFFFu);
    asm volatile("s_sub_u32 %0, %0, 1\n\ts_cbranch_scc0 1f\n\ts_endpgm\n1:" : "+s"(left)::"scc", "memory");
  } while (bad != 0ull);
}
DEVI unsigned clk() { unsigned long long t; asm volatile("s_memrealtime %0\n\ts_waitcnt lgkmcnt(0)" : "=s"(t)::"memory"); return (unsigned)t; }
DEVI void nap_until(unsigned t) { while ((unsigned)(t - clk()) - 1u < 1000u) {} }
DEVI unsigned wait_flags(rsrc_t ws, int off, unsigned want) {
  int left = SPIN_CAP;
  unsigned f;
  for (;;) {
    f = ld4<SC1>(ws, off, 0);
    if (__builtin_amdgcn_ballot_w64(f < want) == 0ull) break;
    asm volatile("s_sub_u32 %0, %0, 1\n\ts_cbranch_scc0 1f\n\ts_endpgm\n1:\n\ts_sleep 2" : "+s"(left)::"scc", "memory");
  }
  __builtin_amdgcn_fence(__ATOMIC_ACQUIRE, "wavefront");
  return f;
}
DEVI void wait_flags_calm(rsrc_t ws, int off, unsigned want) {
  int left = SPIN_CAP;
  for (;;) {
    const unsigned f = ld4<SC1>(ws, off, 0);
    if (__builtin_amdgcn_ballot_w64(f < want) == 0ull) break;
    asm volatile("s_sub_u32 %0, %0, 1\n\ts_cbranch_scc0 1f\n\ts_endpgm\n1:" : "+s"(left)::"scc", "memory");
    if ((unsigned)__builtin_amdgcn_readfirstlane(f) + 2u < want) __builtin_amdgcn_s_sleep(CALM_FAR); else __builtin_amdgcn_s_sleep(CALM_NEAR);
  }
  __builtin_amdgcn_fence(__ATOMIC_ACQUIRE, "wavefront");
}
DEVI unsigned near_mask(rsrc_t ws) {
  const unsigned long long b = __builtin_amdgcn_ballot_w64(ld4<0>(ws, OFF_CTRL + C_NEAR + lane_id() * 4, 0) != 0u);
  return (__builtin_popcountll(b & 0x1FFFFFull) > 10 ? 1u : 0u) | (__builtin_popcountll((b >> 21) & 0x1FFFFFull) > 10 ? 2u : 0u) | (__builtin_popcountll((b >> 42) & 0x1FFFFFull) > 10 ? 4u : 0u);
}
DEVI unsigned latest_seen(rsrc_t ws, int lane, int hb, unsigned& back) {
  int left = SPIN_CAP;
  unsigned v;
  do {
    v = ld4<SC1>(ws, (lane & 3) * 128, hb + 7680);
    asm volatile("s_sub_u32 %0, %0, 1\n\ts_cbranch_scc0 1f\n\ts_endpgm\n1:" : "+s"(left)::"scc", "memory");
  } while (__builtin_amdgcn_ballot_w64(v == 0xFFFFFFFFu) != 0ull);
  const unsigned now = clk() + 256u;
  unsigned d = (now - v) & 0xFFFFFFu, u = v >> 24;
  d = min(d, dppu<0xB1>(d)); d = min(d, dppu<0x4E>(d));
  u = min(u, dppu<0xB1>(u)); u = min(u, dppu<0x4E>(u));
  back = (__builtin_amdgcn_readfirstlane(u) >> 1) + A_OFF;
  return now - __builtin_amdgcn_readfirstlane(d);
}
struct Ann { unsigned a, b, c, d; };
DEVI void ann_issue(const unsigned char* base, Ann& x) {
  asm volatile("s_load_dword %0, %4, 0x0\n\ts_load_dword %1, %4, 0x80\n\ts_load_dword %2, %4, 0x100\n\ts_load_dword %3, %4, 0x180"
               : "=&s"(x.a), "=&s"(x.b), "=&s"(x.c), "=&s"(x.d) : "s"(base) : "memory");
}
DEVI bool ann_latest(Ann& x, unsigned n, unsigned& t, unsigned& back) {
  asm volatile("" : "+s"(x.a), "+s"(x.b), "+s"(x.c), "+s"(x.d));
  t = n - min(min((n - x.a) & 0xFFFFFFu, (n - x.b) & 0xFFFFFFu), min((n - x.c) & 0xFFFFFFu, (n - x.d) & 0xFFFFFFu));
  back = (min(min(x.a, x.b), min(x.c, x.d)) >> 25) + A_OFF;
  return (int)(x.a | x.b | x.c | x.d) >= 0;
}
DEVI int hand_base(int hs, unsigned nm, int s) { return hs + OFF_HAND + s * 65536 + ((int)(nm >> (s >> 5)) & 1) * 8192; }

template <int V> struct IC { static constexpr int value = V; };
template <int N, int I = 0, class Fn> DEVI void static_for(Fn&& f) { if constexpr (I < N) { f(IC<I>{}); static_for<N, I + 1>(f); } }
#define SB() __builtin_amdgcn_sched_barrier(0)
#define PIN_A(x) asm volatile("" : "+a"(x))
#define PIN_V(x) asm volatile("" : "+v"(x))
#define PK4(a) pk(a[0], a[1]), pk(a[2], a[3])
#define SMF_A(acc, m, wt) asm volatile("v_smfmac_f32_16x16x64_bf16 %0, %1, %2, %3" : "+v"(acc) : "v"(m), "a"(wt), "v"(sidx))
#define SMF_V(acc, m, wt) asm volatile("v_smfmac_f32_16x16x64_bf16 %0, %1, %2, %3" : "+v"(acc) : "v"(m), "v"(wt), "v"(sidx))
#define MF32_0(d, a, b) asm volatile("v_mfma_f32_16x16x32_bf16 %0, %1, %2, 0" : "=&v"(d) : "v"(a), "v"(b))
#define MF16_C(d, a, b) asm volatile("v_mfma_f32_16x16x16_bf16 %0, %1, %2, %0" : "+v"(d) : "v"(a), "v"(b))
#define NOPS(n) asm volatile("s_nop " #n)
#define MF32_0A(d, a, b) asm volatile("v_mfma_f32_16x16x32_bf16 %0, %1, %2, 0" : "=&v"(d) : "v"(a), "a"(b))
#define MF32_CA(d, a, b) asm volatile("v_mfma_f32_16x16x32_bf16 %0, %1, %2, %0" : "+v"(d) : "v"(a), "a"(b))
#define MF32_0B(d, a, b) asm volatile("v_mfma_f32_16x16x32_bf16 %0, %1, %2, 0" : "=&v"(d) : "a"(a), "v"(b))
#define MF32_CB(d, a, b) asm volatile("v_mfma_f32_16x16x32_bf16 %0, %1, %2, %0" : "+v"(d) : "a"(a), "v"(b))
template <class FILL> DEVI void proj_seg(f4 (&acc)[3][2], const v4u (&wq)[3][20], v4u (&xa)[4][2], lchar* xb, lchar* xn, FILL fill) {
  static_for<20>([&](auto kc) __attribute__((always_inline)) {
    constexpr int ks = decltype(kc)::value;
    v4u &x0 = xa[ks & 3][0], &x1 = xa[ks & 3][1];
    f4 &a00 = acc[0][0], &a01 = acc[0][1], &a10 = acc[1][0], &a11 = acc[1][1], &a20 = acc[2][0], &a21 = acc[2][1];
    const v4u &w0 = wq[0][ks], &w1 = wq[1][ks], &w2 = wq[2][ks];
    lchar* nx = ks + 2 < 20 ? xb + (ks + 2) * 1024 : xn + (ks - 18) * 1024;
    if constexpr (ks == 0) MF32_0B(a00, w0, x0); else MF32_CB(a00, w0, x0);
    SB(); xa[(ks + 2) & 3][0] = lr16(nx); SB();
    if constexpr (ks == 0) { MF32_0B(a01, w0, x1); MF32_0B(a10, w1, x0); } else { MF32_CB(a01, w0, x1); MF32_CB(a10, w1, x0); }
    SB(); xa[(ks + 2) & 3][1] = lr16(nx + 20480); SB();
    if constexpr (ks == 0) { MF32_0B(a11, w1, x1); MF32_0B(a20, w2, x0); } else { MF32_CB(a11, w1, x1); MF32_CB(a20, w2, x0); }
    SB(); fill(kc); SB();
    if constexpr (ks == 0) MF32_0B(a21, w2, x1); else MF32_CB(a21, w2, x1);
  });
  NOPS(7);
  SB();
}
#define MF32_C(d, a, b) asm volatile("v_mfma_f32_16x16x32_bf16 %0, %1, %2, %0" : "+v"(d) : "v"(a), "v"(b))
#define TP0(d, a, b) do { if constexpr (BA) MF32_0A(d, a, b); else MF32_0(d, a, b); } while (0)
#define TPC(d, a, b) do { if constexpr (BA) MF32_CA(d, a, b); else MF32_C(d, a, b); } while (0)
template <int NT, bool BA, class PUT, class FILL> DEVI void tile_pipe(v4u (&a0)[6], v4u (&a1)[6], const v4u (&b)[NT][6], PUT put, FILL fill) {
  f4 c[2][2];
  v4u o;
  static_for<NT>([&](auto tc) __attribute__((always_inline)) {
    constexpr int t = decltype(tc)::value;
    f4 &c0 = c[t & 1][0], &c1 = c[t & 1][1], &p0 = c[(t + 1) & 1][0], &p1 = c[(t + 1) & 1][1];
    const v4u (&bt)[6] = b[t];
    TP0(c0, a0[0], bt[0]); TP0(c1, a1[0], bt[0]);
    SB(); fill(tc, IC<0>{}); SB();
    TPC(c0, a0[1], bt[1]); TPC(c1, a1[1], bt[1]);
    SB(); fill(tc, IC<1>{}); SB();
    TPC(c0, a0[2], bt[2]);
    SB(); if constexpr (t > 0) { o.x = pk(p0[0], p0[1]); o.y = pk(p0[2], p0[3]); } SB();
    TPC(c1, a1[2], bt[2]);
    SB(); if constexpr (t > 0) { o.z = pk(p1[0], p1[1]); o.w = pk(p1[2], p1[3]); } fill(tc, IC<2>{}); SB();
    TPC(c0, a0[3], bt[3]); TPC(c1, a1[3], bt[3]);
    SB(); if constexpr (t > 0) put(IC<t - 1>{}, o); fill(tc, IC<3>{}); SB();
    TPC(c0, a0[4], bt[4]); TPC(c1, a1[4], bt[4]);
    SB(); fill(tc, IC<4>{}); SB();
    TPC(c0, a0[5], bt[5]); TPC(c1, a1[5], bt[5]);
    SB(); fill(tc, IC<5>{}); SB();
  });
  NOPS(7);
  SB();
  put(IC<NT - 1>{}, v4u{PK4(c[(NT - 1) & 1][0]), PK4(c[(NT - 1) & 1][1])});
  SB();
}
DEVI float row16_max_scale(float x, float& a, float& b, float& c_, float& d, float c) {
  asm volatile("v_mul_f32 %1, %5, %1\n\tv_mul_f32 %2, %5, %2\n\tv_max_f32_dpp %0, %0, %0 quad_perm:[1,0,3,2] row_mask:0xf bank_mask:0xf\n\t"
               "v_mul_f32 %3, %5, %3\n\tv_mul_f32 %4, %5, %4\n\tv_max_f32_dpp %0, %0, %0 quad_perm:[2,3,0,1] row_mask:0xf bank_mask:0xf\n\t"
               "s_nop 1\n\tv_max_f32_dpp %0, %0, %0 row_half_mirror row_mask:0xf bank_mask:0xf\n\t"
               "s_nop 1\n\tv_max_f32_dpp %0, %0, %0 row_mirror row_mask:0xf bank_mask:0xf" : "+v"(x), "+v"(a), "+v"(b), "+v"(c_), "+v"(d) : "s"(c));
  return x;
}
DEVI void dma_sc1(v4u srd, unsigned lds, int voff, int soff) {
  asm volatile("s_mov_b32 m0, %0\n\ts_nop 0\n\tbuffer_load_dwordx4 %1, %2, %3 offen sc1 lds" ::"s"(lds), "v"(voff), "s"(srd), "s"(soff) : "memory");
}
DEVI void dma_1k(v4u srd, unsigned lds, int voff, int soff) {
  asm volatile("s_mov_b32 m0, %0\n\ts_nop 0\n\tbuffer_load_dwordx4 %1, %2, %3 offen lds" ::"s"(lds), "v"(voff), "s"(srd), "s"(soff) : "memory");
}
DEVI v4u mk_srd(const void* p, int bytes) { const unsigned long long a = (unsigned long long)p; return v4u{(unsigned)a, (unsigned)(a >> 32) & 0xFFFFu, (unsigned)bytes, 0x00020000u}; }
template <int N> DEVI void vmwait() { asm volatile("s_waitcnt vmcnt(%0)" ::"i"(N) : "memory"); }

template <int NG, bool SPARSE, int RSB, int GAP, class SRD, class SO, class PICK, class SINK> DEVI void stream_proj(lchar* L, int lane, int w, SRD srd, SO so, PICK pick, SINK sink) {
  int vo[3];
#pragma unroll
  for (int i = 0; i < 3; ++i) {
    const int n = 64 * i + lane, row = n / 24, c = n % 24, sw = 2 * (((row >> (SPARSE ? 2 : 1)) & 1) | ((w & 1) << 1));
    vo[i] = (8 * w + row) * RSB + ((c & ~7) | ((c & 7) ^ sw)) * 16;
  }
  lchar* ra[3];
  {
    const int g = lane >> 4, q = (lane & 15) >> 2, pp = lane & 3;
    const int row = SPARSE ? 16 * (g >> 1) + 4 * (g & 1) + (q & 1) + 8 * (q >> 1) : 8 * g + q;
    const int sw = 2 * (((row >> (SPARSE ? 2 : 1)) & 1) | (((row >> 3) & 1) << 1));
#pragma unroll
    for (int t = 0; t < 3; ++t) {
      const int c = 2 * (t < 2 ? 2 * w + t : 8 + w) + (pp >> 1);
      ra[t] = L + row * 384 + ((c & ~7) | ((c & 7) ^ sw)) * 16 + (pp & 1) * 8;
    }
  }
  const unsigned lb = (unsigned)(size_t)L + w * 3072;
  auto issue = [&](auto gc) __attribute__((always_inline)) {
    constexpr int g = decltype(gc)::value;
    static_for<4>([&](auto cc) __attribute__((always_inline)) {
      constexpr int c = decltype(cc)::value;
      const int b = so(gc, cc);
#pragma unroll
      for (int i = 0; i < 3; ++i) dma_1k(srd(gc, cc), lb + ((g % 3) * 4 + c) * 12288 + i * 1024, vo[i], b);
      if constexpr (GAP > 0) __builtin_amdgcn_s_sleep(GAP);
    });
  };
  asm volatile("s_nop 4");
  static_for<3>([&](auto gc) __attribute__((always_inline)) { issue(gc); });
  static_for<NG>([&](auto gc) __attribute__((always_inline)) {
    constexpr int g = decltype(gc)::value;
    vmwait<(NG - 1 - g < 2 ? NG - 1 - g : 2) * 12>();
    BAR();
    v4u o[4][3];
    static_for<4>([&](auto cc) __attribute__((always_inline)) {
      constexpr int c = decltype(cc)::value;
      const int off = ((g % 3) * 4 + pick(gc, cc)) * 12288;
#pragma unroll
      for (int t = 0; t < 3; ++t) { const v2u x0 = lrtr(ra[t] + off), x1 = lrtr(ra[t] + off + (SPARSE ? 2 : 4) * 384); o[c][t] = v4u{x0.x, x0.y, x1.x, x1.y}; }
    });
    BAR();
    if constexpr (g + 3 < NG) issue(IC<g + 3>{});
    static_for<4>([&](auto cc) __attribute__((always_inline)) { sink(gc, cc, o[decltype(cc)::value]); });
    SB();
  });
}

DEVI void ln_stats(v4u (&x)[2][5], LAS f4* r, int w, int lane) {
  v4u ones = {0x3F803F80u, 0x3F803F80u, 0x3F803F80u, 0x3F803F80u};
  f4 a0, q0, a1, q1;
  PIN_V(ones);
#pragma unroll
  for (int k = 0; k < 5; ++k) { PIN_V(x[0][k]); PIN_V(x[1][k]); }
  NOPS(1);
  MF32_0(a0, ones, x[0][0]); MF32_0(q0, x[0][0], x[0][0]); MF32_0(a1, ones, x[1][0]); MF32_0(q1, x[1][0], x[1][0]);
#pragma unroll
  for (int k = 1; k < 5; ++k) { MF32_C(a0, ones, x[0][k]); MF32_C(q0, x[0][k], x[0][k]); MF32_C(a1, ones, x[1][k]); MF32_C(q1, x[1][k], x[1][k]); }
  NOPS(7);
  PIN_V(a0); PIN_V(q0); PIN_V(a1); PIN_V(q1);
  const int c = lane & 3;
  const f4 o = {a0[0], c == 0 ? q0[0] : c == 1 ? q0[1] : c == 2 ? q0[2] : q0[3], a1[0], c == 0 ? q1[0] : c == 1 ? q1[1] : c == 2 ? q1[2] : q1[3]};
  if ((lane >> 4) == ((lane & 15) >> 2)) r[w * 16 + (lane & 15)] = o;
}
DEVI f4 ln_finish(LAS f4* r, int row) {
  const f4 x = ((r[row] + r[16 + row]) + (r[32 + row] + r[48 + row])) * (1.f / D_);
  const float r0 = __builtin_amdgcn_rsqf(fmaxf(x[1] - x[0] * x[0], 0.f) + 1e-5f), r1 = __builtin_amdgcn_rsqf(fmaxf(x[3] - x[2] * x[2], 0.f) + 1e-5f);
  return f4{r0, -x[0] * r0, r1, -x[2] * r1};
}
__global__ void __launch_bounds__(256) k0_prep(Params p) {
  __shared__ __attribute__((aligned(16))) float red_[128 * 16];
  LAS float* red = (LAS float*)red_;
  const rsrc_t ws = mkrs(p.ws, WS_BYTES);
  const int hs = hand_shift(p.ws);
  const int tid = threadIdx.x, lane = tid & 63, b = blockIdx.x;
  const v4u poison = {0xFFFFFFFFu, 0xFFFFFFFFu, 0xFFFFFFFFu, 0xFFFFFFFFu};
  if (b < 240) {
    const int cg = tid & 1, rg = tid >> 1;
    v4u wv[5];
    float cv[5];
#pragma unroll
    for (int i = 0; i < 5; ++i) { wv[i] = *(const GAS v4u*)(p.wmod + (size_t)(rg * 5 + i) * (6 * D_) + ((b & 7) * 30 + (b >> 3)) * 16 + cg * 8); cv[i] = b2f(p.c[rg * 5 + i]); }
    SB();
#pragma unroll
    for (int i = 0; i < NPOI; ++i) {
      const int pc = b + 240 * i;
      st16<P_AUX>(poison, ws, pc < 320 ? hs + OFF_YR + pc * 4096 + tid * 16 : pc < 1328 ? hs + OFF_HAND + (pc - 304) * 4096 + tid * 16 :
                  pc < 1348 ? hs + OFF_PT + (NSEG - 1) * 81920 + (pc - 1328) * 4096 + tid * 16 : pc < 1358 ? OFF_X1 + (NSEG - 1) * 40960 + (pc - 1348) * 4096 + tid * 16 : 0x30000000, 0);
    }
    float acc[8] = {0, 0, 0, 0, 0, 0, 0, 0};
#pragma unroll
    for (int i = 0; i < 5; ++i) {
      const float sv = rb(cv[i] * __builtin_amdgcn_rcpf(1.f + __expf(-cv[i])));
#pragma unroll
      for (int j = 0; j < 4; ++j) { acc[2 * j] += sv * blo(wv[i][j]); acc[2 * j + 1] += sv * bhi(wv[i][j]); }
    }
    *(LAS f4*)(red + tid * 8) = f4{acc[0], acc[1], acc[2], acc[3]}; *(LAS f4*)(red + tid * 8 + 4) = f4{acc[4], acc[5], acc[6], acc[7]};
    __syncthreads();
    if (tid < 64) {
      float s = 0;
#pragma unroll
      for (int i = 0; i < 32; ++i) s += red[((lane >> 4) * 32 + i) * 16 + (lane & 15)];
      s = xsum16_32(s);
      st4<0>(pk(s, dppf<0xB1>(s)), ws, (lane < 16 && !(lane & 1)) ? OFF_CTRL + C_MOD + (((b & 7) * 30 + (b >> 3)) * 16 + lane) * 2 : 0x30000000, 0);
    }
    return;
  }
  if (b < 256) {
    unsigned best[2] = {0xFFFFFFFFu, 0xFFFFFFFFu};
    const int blk = (b - 240) * 4 + (tid >> 6);
    const int pb = hs + (blk < 42 ? OFF_HAND + (blk / 21) * 0x200000 + (1 + blk % 21) * 65536 : OFF_YR + 2 * YRS + (blk % 11) * 65536);
#pragma unroll 1
    for (int i = 0; i < NPROBE; ++i) {
      unsigned long long c0, c1;
      asm volatile("s_memtime %0\n\ts_waitcnt lgkmcnt(0)" : "=s"(c0)::"memory");
      st4<SC1>(0u, ws, lane == 0 ? pb + (1 + (i >> 1)) * 16384 + (i & 1) * 8192 + 7936 : 0x30000000, 0);
      VMWAIT(0);
      asm volatile("s_memtime %0\n\ts_waitcnt lgkmcnt(0)" : "=s"(c1)::"memory");
      const unsigned dt = (unsigned)(c1 - c0);
      if (i & 1) best[1] = min(best[1], dt); else best[0] = min(best[0], dt);
    }
    const unsigned nearhalf = (best[1] < best[0] ? 1u : 0u) ^ (xcc_id() >> 2);
    st4<0>(nearhalf, ws, lane == 0 ? OFF_CTRL + C_NEAR + blk * 4 : 0x30000000, 0);
  }
  else {
    if (b < 268) st16<0>(v4u{0, 0, 0, 0}, ws, OFF_CTRL + (b - 256) * 4096 + tid * 16, 0);
    const int n = (b - 256) * 256 + tid, o = (n * 16) & 8191, ch = n >> 10;
    v4u v = poison;
    if (o >= 5120 && o < 7680) {
      const int q = (o - 5120) >> 7, r = q < 16 ? ch * 16 + q : 64 + ch * 4 + q - 16;
      const v4u x = *(const GAS v4u*)(p.mem0 + (n & 7) * D_ + 8 * r);
      v = v4u{nosent(x.x), nosent(x.y), nosent(x.z), nosent(x.w)};
    }
    st16<0>(v, ws, hs + OFF_HAND + n * 16, 0);
  }
}

DEVI void h_seg(const Params& p, rsrc_t ws, lchar* Ls, int w, int k) {
  const int lane = lane_id(), tid = w * 64 + lane, row = tid & 15, part = tid >> 4;
  v4u xv[2][5], sa[5], ca[5];
#pragma unroll
  for (int n = 0; n < 2; ++n)
#pragma unroll
    for (int i = 0; i < 5; ++i) xv[n][i] = *(const GAS v4u*)(p.x + (size_t)(k * 32 + n * 16 + row) * D_ + (part + 16 * i) * 8);
#pragma unroll
  for (int i = 0; i < 5; ++i) { sa[i] = ld16<0>(ws, (part + 16 * i) * 16, OFF_CTRL + C_MOD); ca[i] = ld16<0>(ws, (part + 16 * i) * 16, OFF_CTRL + C_MOD + D_ * 2); }
  SB();
  ln_stats(xv, (LAS f4*)Ls, w, lane);
  BAR();
  const f4 n1 = ln_finish((LAS f4*)Ls, row);
#pragma unroll
  for (int n = 0; n < 2; ++n)
#pragma unroll
    for (int i = 0; i < 5; ++i) {
      v4u o;
#pragma unroll
      for (int j = 0; j < 4; ++j) {
        const f2 y = (f2{blo(xv[n][i][j]), bhi(xv[n][i][j])} * n1[2 * n] + n1[2 * n + 1]) * f2{1.f + blo(ca[i][j]), 1.f + bhi(ca[i][j])} + f2{blo(sa[i][j]), bhi(sa[i][j])};
        o[j] = pk(y.x, y.y);
      }
      st16<SC1>(o, ws, OFF_HF + k * 40960 + n * 20480 + (part + 16 * i) * 256 + row * 16, 0);
    }
  VMWAIT(0);
  BAR();
  if (w == 0) st4<SC1>(1u, ws, lane == 0 ? OFF_CTRL + C_FLH + k * 4 : 0x30000000, 0);
}

constexpr int A_W = 0, A_M = 131072, A_PM = A_M + 10240, A_PT = A_PM + 4 * 1280, A_END = A_PT + 4 * 4096;
static_assert(A_END + 1024 <= 163840, "lds");

DEVI void role_a(const Params& p, rsrc_t ws, lchar* L, int w, int h) {
  const int hs = hand_shift(p.ws);
  const int lane = lane_id(), g = lane >> 4;
  v8u wr[44];
  lchar* Lw = L + A_W + w * 32768 + lane * 16;
  {
    const v4u sq = mk_srd(p.Wq, D_ * HE * 2), sk = mk_srd(p.Wk, D_ * HE * 2);
    v4u lo[4][3], wt[16][2];
    stream_proj<10, true, HE * 2, 0>(L, lane, w,
      [&](auto gc, auto cc) __attribute__((always_inline)) { constexpr int gi = decltype(gc)::value, c = decltype(cc)::value; return (gi < 8 ? gi >> 2 : c >> 1) ? sk : sq; },
      [&](auto gc, auto cc) __attribute__((always_inline)) {
        constexpr int gi = decltype(gc)::value, c = decltype(cc)::value, fr = gi < 8 ? 2 * c + ((gi >> 1) & 1) : 8 + (c & 1);
        return (2 * fr + (gi & 1)) * 32 * (HE * 2) + h * (E_ * 2);
      },
      [&](auto gc, auto cc) __attribute__((always_inline)) { return decltype(gc)::value < 8 ? (decltype(cc)::value ^ w) : decltype(cc)::value; },
      [&](auto gc, auto cc, v4u (&o)[3]) __attribute__((always_inline)) {
        constexpr int gi = decltype(gc)::value, c = decltype(cc)::value;
        constexpr int j = gi < 8 ? 2 * c + ((gi >> 1) & 1) : 8 + (c & 1), q = gi < 8 ? gi >> 2 : c >> 1;
        static_for<3>([&](auto tc) __attribute__((always_inline)) {
          constexpr int t = decltype(tc)::value, n = 16 * t + 2 * (j - 2) + q;
          if constexpr (j >= 2 && n % 3 == 2) wt[n / 3][gi & 1] = o[t];
          else if constexpr ((gi & 1) == 0) lo[c][t] = o[t];
          else {
            constexpr int ri = j < 2 ? j * 6 + 2 * t + q : 12 + n - (n + 1) / 3;
            v8u x = cat(lo[c][t], o[t]);
            if constexpr (ri < 32) PIN_A(x); else PIN_V(x);
            wr[ri] = x;
          }
        });
      });
#pragma unroll
    for (int i = 0; i < 16; ++i) { lw16(Lw + i * 2048, wt[i][0]); lw16(Lw + i * 2048 + 1024, wt[i][1]); }
  }
  const int sidx = (lane & 8) ? 0xEEEE : 0x4444;
  const unsigned nm = near_mask(ws);
  lchar* Lm_own = L + A_M + w * 2048 + lane * 16;
  lchar* Lm_half = L + A_M + 8192 + w * 512 + lane * 8;
  lchar* Lm1 = L + A_M + (w ^ 1) * 2048 + lane * 16;
  lchar* Lm2 = L + A_M + (w ^ 2) * 2048 + lane * 16;
  lchar* Lm3 = L + A_M + (w ^ 3) * 2048 + lane * 16;
  lchar* Lm8 = L + A_M + 8192 + lane * 16;
  lchar* Lpm_w = L + A_PM + w * 1280 + (lane & 7) * 160 + 32 + g * 32 + ((lane >> 3) & 1) * 16;
  lchar* Lpm_m = g >= 2 ? L + A_PM + w * 1280 + (lane & 7) * 160 + (g - 2) * 16 + ((lane >> 3) & 1) * 8 : L + A_END + w * 256 + lane * 8;
  const int u = min(lane & 15, 9);
  lchar* Lpm_r = L + A_PM + ((w & 1) * 4 + g) * 160 + u * 16;
  constexpr int OOB = 0x30000000;
  const int pst = (w < 2 && (lane & 15) < 10 && (lane & 1) == 0) ? (h >> 3) * 16384 + (u >> 1) * 1024 + (h & 7) * 128 + ((w & 1) * 4 + g) * 16 : OOB;
  const int flb = OFF_CTRL + C_FLB + (h * 3 + (lane & 1)) * 128 + w * 32;
  const int qkoff = lane * 16, qkb = OFF_QK + h * QH + w * 3072;
  v4u sel;
  {
    const int n = lane & 15, kb = 4 * (g & 1), e = (n & 7) - kb;
    const unsigned lo = e == 0 ? 0x3F80u : e == 1 ? 0x3F800000u : 0u, hi = e == 2 ? 0x3F80u : e == 3 ? 0x3F800000u : 0u;
    sel = n < 8 ? v4u{lo, hi, 0u, 0u} : v4u{0u, 0u, lo, hi};
  }
  const int ts1 = (w & 1) * 32768 + h * 1024 + lane * 16;
  const int ts2 = lane < 16 ? 65536 + (w & 1) * 8192 + h * 256 + lane * 16 : OOB;
  lchar* Lpark = L + A_PT + w * 4096 + 1024 + (w & 1) * 2048 + lane * 16;
  lchar* Lpark2 = L + A_PT + w * 4096 + (w & 1) * 512 + (lane & 15) * 16;
  const int flp = (lane == 0 && w >= 2) ? OFF_CTRL + C_FLP + h * 64 + (w & 1) * 4 : OOB;
  const int climit = __builtin_amdgcn_readfirstlane(w < 2 ? NSEG - 1 : 0);
  const unsigned qlimit = __builtin_amdgcn_readfirstlane(w < 2 ? 0 : LATE);
  const bool ancu = h % 9 == 0;
  const int tq = (w >> 1) * 8 + (lane & 7), tsel = (lane >> 3) & 1;
  lchar* Ltk = L + A_PT + 1024 + (w & 1) * (2048 - tsel * 1024) + (g * 16 + tq) * 16;
  lchar* Ltm = L + A_PT + (w & 1) * 512 + (tsel * 16 + tq) * 16 + g * 4;
  lchar* Lpw = L + A_PT + (2 + (w & 1)) * 4096 + 1024 + (w & 1) * 2048 + (g * 16 + tq) * 16 + (tsel ^ (w & 1)) * 8;
  lchar* Lpw2 = L + A_PT + (2 + (w & 1)) * 4096 + (w & 1) * 512 + tq * 16 + (tsel * 4 + g) * 2;
  const int nvis = tsel ? ((w & 1) ? 4 : 0) : tq - 4 * g + 1;
  auto put_tok = [&](int s) __attribute__((always_inline)) {
    const v4u a = lr16(Lpark), b = lr16(Lpark2);
    st16<SC1>(a, ws, ts1, hs + OFF_PT + s * 81920); st16<SC1>(b, ws, ts2, hs + OFF_PT + s * 81920);
  };
  constexpr float CS = 0.07216878364870322f * 1.4426950408889634f;
  constexpr float NEG = -1.0e9f * 13.856406460551018f;
  const float lbias = (lane & 15) < 10 ? 0.f : __builtin_inff();

  VMWAIT(0);
  wait_flags(ws, flb, 1u);
  unsigned prog = 0, prog_n = 0, dur8 = DUR0 * 8, tnap = clk();
  const int tsoff = (lane == 1 && h == 9 * w) ? 7680 + w * 128 : OOB;
  v4u kt[3], qt[3];
#pragma unroll
  for (int i = 0; i < 3; ++i) { kt[i] = ld16<SC1>(ws, qkoff + i * 1024, qkb + 12288); qt[i] = ld16<SC1>(ws, qkoff + i * 1024, qkb); }

  f4 acc[6];
  v8u wl[4];
  int pst_s, ao;
  unsigned av;
  auto setup = [&](int s) __attribute__((always_inline)) {
    const int hb = hand_base(hs, nm, s);
#pragma unroll
    for (int i = 0; i < 4; ++i) wl[i] = cat(lr16(Lw + i * 2048), lr16(Lw + i * 2048 + 1024));
#pragma unroll
    for (int i = 0; i < 6; ++i) {
      f2 a, b;
      asm volatile("v_mov_b64 %0, 0\n\tv_mov_b64 %1, 0" : "=v"(a), "=v"(b));
      acc[i] = f4{a.x, a.y, b.x, b.y};
    }
    pst_s = pst + hb;
    ao = lane == 0 ? (s > LATE ? flp : OOB) : tsoff + hb;
    av = (unsigned)s;
    { int &x = pst_s, &y = ao; unsigned& z = av; PIN_V(x); PIN_V(y); PIN_V(z); }
    SB();
  };
  setup(0);
#pragma unroll 1
  for (int s = 0; s < NSEG; ++s) {
    const int hb = hand_base(hs, nm, s);
    v4u m[10];
    unsigned long long tseen;
    {
      v2u v2;
      int so = hb + w * 16384 + 5120;
      asm volatile("" : "+s"(so));
      nap_until(tnap);
      m[0] = ld16<SC1>(ws, lane * 16, so); m[1] = ld16<SC1>(ws, lane * 16 + 1024, so); v2 = ld8<SC1>(ws, lane * 8 + 2048, so);
      SB();
      lw16(Lm_own, m[0]);
      SB();
      SMF_A(acc[0], m[0], wr[0]);
      unsigned mm = mx4(0u, m[0]);
      SB();
      SMF_A(acc[1], m[0], wr[1]);
      SB();
      lw16(Lm_own + 1024, m[1]);
      SB();
      SMF_A(acc[2], m[0], wr[2]);
      mm = mx4(mm, m[1]);
      SB();
      SMF_A(acc[3], m[0], wr[3]);
      SB();
      lw8(Lm_half, v2);
      PIN_V(prog_n);
      asm volatile("s_waitcnt vmcnt(0)\n\ts_memrealtime %0" : "=s"(tseen)::"memory");
      SB();
      SMF_A(acc[4], m[0], wr[4]);
      mm = mx3(mm, v2.x, v2.y);
      const unsigned long long bad = __builtin_amdgcn_ballot_w64(mm == 0xFFFFFFFFu);
      SB();
      SMF_A(acc[5], m[0], wr[5]);
      SB();
      if (__builtin_expect(bad != 0ull, 0)) {
        asm volatile("s_waitcnt lgkmcnt(0)" : "+s"(tseen)::"memory");
        gather_m(ws, lane, so, m[0], m[1], v2);
        tseen = clk() + SLOW_ADD;
        VMWAIT(0);
        lw16(Lm_own, m[0]); lw16(Lm_own + 1024, m[1]); lw8(Lm_half, v2);
#pragma unroll
        for (int i = 0; i < 6; ++i) {
          f2 a, b;
          asm volatile("v_mov_b64 %0, 0\n\tv_mov_b64 %1, 0" : "=v"(a), "=v"(b));
          acc[i] = f4{a.x, a.y, b.x, b.y};
          PIN_V(acc[i]);
        }
        PIN_V(m[0]);
        NOPS(1);
#pragma unroll
        for (int n = 0; n < 6; ++n) SMF_A(acc[n], m[0], wr[n]);
        NOPS(7);
#pragma unroll
        for (int n = 0; n < 6; ++n) PIN_V(acc[n]);
        NOPS(1);
      }
    }
    SB();
    SMF_A(acc[0], m[1], wr[6]); SMF_A(acc[1], m[1], wr[7]);
    SB();
    BAR();
    m[2] = lr16(Lm1); m[3] = lr16(Lm1 + 1024);
    SB();
    SMF_A(acc[2], m[1], wr[8]);
    SB();
    m[4] = lr16(Lm2); m[5] = lr16(Lm2 + 1024);
    SB();
    SMF_A(acc[3], m[1], wr[9]);
    SB();
    m[6] = lr16(Lm3); m[7] = lr16(Lm3 + 1024);
    SB();
    SMF_A(acc[4], m[1], wr[10]);
    SB();
    m[8] = lr16(Lm8); m[9] = lr16(Lm8 + 1024);
    SB();
    SMF_A(acc[5], m[1], wr[11]);
    asm volatile("" : "+s"(tseen));
    SB();
    f4 sM, s0, s1, ft;
    unsigned long long tpub = 0;
    asm volatile("" : "+s"(tpub));
    v4u pa, F01;
    v2u F2;
#pragma unroll
    for (int n = 0; n < 48; ++n) {
      const int t = n / 16, i = n % 16, tl = 2 * t + (n & 1), j = 2 + i / 2, ri = 12 + n - (n + 1) / 3;
      if (n % 3 == 2) {
        SMF_V(acc[tl], m[j], wl[(n / 3) % 4]);
        SB();
        if (n / 3 + 4 < 16) wl[(n / 3) % 4] = cat(lr16(Lw + (n / 3 + 4) * 2048), lr16(Lw + (n / 3 + 4) * 2048 + 1024));
      } else if (ri < 32) SMF_A(acc[tl], m[j], wr[ri]);
      else SMF_V(acc[tl], m[j], wr[ri]);
      SB();
      if (t >= 1) {
        if (i == 4) { pa.x = pk(acc[2 * t - 2][0], acc[2 * t - 2][1]); pa.y = pk(acc[2 * t - 2][2], acc[2 * t - 2][3]); }
        if (i == 5) { pa.z = pk(acc[2 * t - 1][0], acc[2 * t - 1][1]); pa.w = pk(acc[2 * t - 1][2], acc[2 * t - 1][3]); }
        if (i == 7) MF32_0(ft, pa, sel);
        if (i == 12) { F01[2 * t - 2] = pk(ft[0], ft[1]); F01[2 * t - 1] = pk(ft[2], ft[3]); }
      }
      if (n == 0) {
        asm volatile("v_writelane_b32 %0, %1, 1" : "+v"(av) : "s"((((unsigned)tseen + (dur8 >> 3)) & 0xFFFFFFu) | (min(dur8 >> 3, 127u) << 24)));
        st4<SC1>(av, ws, ao, 0);
      }
      SB();
    }
    MF32_0(s0, kt[0], F01);
    MF32_0(s1, kt[1], F01);
    SB();
    pa.x = pk(acc[4][0], acc[4][1]); pa.y = pk(acc[4][2], acc[4][3]);
    SB();
    NOPS(3);
    SB();
    pa.z = pk(acc[5][0], acc[5][1]); pa.w = pk(acc[5][2], acc[5][3]);
    SB();
    NOPS(1);
    MF32_0(ft, pa, sel);
    MF32_0(sM, F01, F01);
    NOPS(6);
    SB();
    F2 = v2u{PK4(ft)};
    SB();
    NOPS(1);
    MF16_C(s1, hi2(kt[2]), F2);
    MF16_C(s0, lo2(kt[2]), F2);
    MF16_C(sM, F2, F2);
    NOPS(6);
    SB();
#pragma unroll
    for (int i = 0; i < 4; ++i) s0[i] = uf((unsigned)__builtin_amdgcn_update_dpp((int)fu(s0[i]), (int)fu(s1[i]), 0x118, 0xF, 0xC, false));
    lwf4(Lpm_w, s0);
    lw8(Lpm_m, v2u{(unsigned)__builtin_amdgcn_update_dpp((int)fu(sM[0]), (int)fu(sM[2]), 0x118, 0xF, 0xC, false),
                   (unsigned)__builtin_amdgcn_update_dpp((int)fu(sM[1]), (int)fu(sM[3]), 0x118, 0xF, 0xC, false)});
    SB();
    BAR();
    if (__builtin_expect(s < climit, 1)) {
      const f4 y0 = lrf4(Lpm_r), y1 = lrf4(Lpm_r + 1280), y2 = lrf4(Lpm_r + 2560), y3 = lrf4(Lpm_r + 3840);
      SB();
      const f4 x = (y0 + y1) + (y2 + y3);
      float e[4] = {x[0], x[1], x[2], x[3]};
      const float ms = __builtin_fmaf(row16_max_scale(fmaxf(fmaxf(x[0], x[1]), fmaxf(x[2], x[3])), e[0], e[1], e[2], e[3], CS), CS, lbias);
#pragma unroll
      for (int i = 0; i < 4; ++i) { e[i] = __builtin_amdgcn_exp2f(e[i] - ms); PIN_V(e[i]); }
      float sa = e[0] + e[1], sb = e[2] + e[3];
      PIN_V(sa);
      float sum = sa + sb;
      sum += dppf<0xB1>(sum); sum += dppf<0x4E>(sum); sum += dppf<0x141>(sum); sum += dppf<0x140>(sum);
      const float rc = __builtin_amdgcn_rcpf(sum);
      unsigned a0, a1;
      f2 p0 = f2{e[0], e[1]} * rc, p1 = f2{e[2], e[3]} * rc;
      asm("" : "+v"(p0), "+v"(p1));
      asm("v_cvt_pk_bf16_f32 %0, |%2|, |%3|\n\tv_cvt_pk_bf16_f32 %1, |%4|, |%5|" : "=&v"(a0), "=v"(a1) : "v"(p0.x), "v"(p0.y), "v"(p1.x), "v"(p1.y));
      const unsigned n0 = dppu<0xB1>(a0), n1 = dppu<0xB1>(a1);
      st16<SC1>(v4u{a0, a1, n0, n1}, ws, pst_s, 0);
      SB();
      asm volatile("s_memrealtime %0" : "=s"(tpub)::"memory");
    }
    if ((unsigned)(s - 1) < qlimit) {
      st4<SC1>((unsigned)s - 1u, ws, flp, 0);
      put_tok(s - 1);
    }
    SB();
    SB();
    prog = max(prog, prog_n);
    const bool qk_ok = __builtin_amdgcn_ballot_w64(prog < (unsigned)min(s + 2, NSEG)) == 0ull, qk_far = __builtin_amdgcn_ballot_w64(prog < (unsigned)min(s + 5, NSEG)) == 0ull;
    Ann ann;
    ann_issue(p.ws + hb + 7680, ann);
    {
      f4 tm0, t00, tm1, t10, t11;
      MF32_0(tm0, F01, qt[0]); MF32_0(t00, kt[0], qt[0]); MF32_0(tm1, F01, qt[1]); MF32_0(t10, kt[0], qt[1]); MF32_0(t11, kt[1], qt[1]);
      MF16_C(tm0, F2, lo2(qt[2])); MF16_C(t00, lo2(kt[2]), lo2(qt[2])); MF16_C(tm1, F2, hi2(qt[2])); MF16_C(t10, lo2(kt[2]), hi2(qt[2])); MF16_C(t11, hi2(kt[2]), hi2(qt[2]));
      NOPS(7);
      SB();
      lchar* d = L + A_PT + w * 4096 + lane * 16;
#pragma unroll
      for (int i = 0; i < 4; ++i) tm0[i] = uf(__builtin_amdgcn_permlane32_swap(fu(tm0[i]), fu(tm1[i]), false, false)[1]);
      lwf4(d, tm0); lwf4(d + 1024, t00); lwf4(d + 2048, t10); lwf4(d + 3072, t11);
    }
    auto next_qk = [&]() __attribute__((always_inline)) {
      if (__builtin_expect(!qk_ok, 0)) wait_flags(ws, flb, (unsigned)min(s + 2, NSEG));
      const int so = qkb + min(s + 1, NSEG - 1) * QS;
#pragma unroll
      for (int i = 0; i < 3; ++i) { kt[i] = ld16<SC1>(ws, qkoff + i * 1024, so + 12288); qt[i] = ld16<SC1>(ws, qkoff + i * 1024, so); }
      prog_n = ld4<SC1>(ws, qk_far ? OOB : flb, 0);
      SB();
    };
    SB();
    asm volatile("s_waitcnt lgkmcnt(0)" : "+s"(tpub)::"memory");
    if (w >= 2) next_qk();
    else if (ancu && w == 0) *(LAS unsigned*)(L + A_PM) = (unsigned)tpub;
    BAR();
    {
      f4 r[4];
      float rm[4];
#pragma unroll
      for (int i = 0; i < 4; ++i) { r[i] = lrf4(Ltk + i * 4096); rm[i] = *(LAS float*)(Ltm + i * 4096); }
      SB();
      if (w < 2) next_qk();
      setup(min(s + 1, NSEG - 1));
      { unsigned back; if (ann_latest(ann, (unsigned)tseen + 1024u, tnap, back)) tnap += back; }
      SB();
      f4 x = (r[0] + r[1]) + (r[2] + r[3]);
      const float xm = (rm[0] + rm[1]) + (rm[2] + rm[3]);
#pragma unroll
      for (int i = 0; i < 4; ++i) x[i] = i < nvis ? x[i] : NEG;
      float mx = fmaxf(fmaxf(fmaxf(x[0], x[1]), fmaxf(x[2], x[3])), xm);
      mx = fmaxf(mx, dppf<0x128>(mx));
      { const auto t = __builtin_amdgcn_permlane16_swap(fu(mx), fu(mx), false, false); mx = fmaxf(uf(t[0]), uf(t[1])); }
      { const auto t = __builtin_amdgcn_permlane32_swap(fu(mx), fu(mx), false, false); mx = fmaxf(uf(t[0]), uf(t[1])); }
      x = (x - mx) * CS;
#pragma unroll
      for (int i = 0; i < 4; ++i) x[i] = __builtin_amdgcn_exp2f(x[i]);
      const float em = __builtin_amdgcn_exp2f((xm - mx) * CS);
      float sum = ((x[0] + x[1]) + (x[2] + x[3])) + em;
      sum += dppf<0x128>(sum);
      const float rc = __builtin_amdgcn_rcpf(xsum16_32(sum));
#define PKA(a, b) pk(__builtin_fabsf((a) * rc), __builtin_fabsf((b) * rc))
      lw8(Lpw, v2u{PKA(x[0], x[1]), PKA(x[2], x[3])});
      *(LAS u16*)Lpw2 = (u16)PKA(em, em);
      if (__builtin_expect(s >= LATE, 0)) { BAR(); if (w >= 2 && s + 1 < NSEG) put_tok(s); }
    }
    if (h == 9 * w) {
      const unsigned d = __builtin_amdgcn_readfirstlane(*(LAS unsigned*)(L + A_PM)) - (unsigned)tseen;
      dur8 += min(max((int)(d * 8u - dur8), -96), 32) >> (s < 8 ? 1 : 2);
    }
  }
  if (w >= 2) {
    const int o = hs + OFF_PT + (NSEG - 1) * 81920 + ((((w & 1) * 2 + ((lane >> 3) & 1)) * 4 + (h >> 3)) * 5) * 1024 + (h & 7) * 128 + (lane & 7) * 16;
    st16<SC1>(lr16(Lpark), ws, o + (1 + g) * 1024, 0);
    st16<SC1>(lr16(Lpark2), ws, lane < 16 ? o : OOB, 0);
  }
}

DEVI void role_c1(const Params& p, rsrc_t ws, lchar* L, int w, int r) {
  const int hs = hand_shift(p.ws);
  const int lane = lane_id(), g = lane >> 4, rho = lane & 15;
  const int h = 2 * (r / 5) + (w >> 1), cg = r % 5;
  const v4u poison = {0xFFFFFFFFu, 0xFFFFFFFFu, 0xFFFFFFFFu, 0xFFFFFFFFu};
  int yown = hs + OFF_YR + (r >> 1) * 16384 + (r & 1) * 4096 + w * 1024 + lane * 16;
  v8u u[4][10];
  {
    const v4u sv = mk_srd(p.Wv, D_ * HE * 2), so_ = mk_srd(p.Wo, D_ * HE * 2);
    const unsigned l0 = (unsigned)(size_t)L;
    asm volatile("s_nop 4");
#pragma unroll
    for (int i = 0; i < 24; ++i)
      dma_1k(so_, l0 + (w * 24 + i) * 1024, g * (D_ * 2) + ((rho ^ (2 * g) ^ (((i >> 1) & 1) * 8)) * 16), (h * E_ + (w & 1) * 96 + i * 4) * (D_ * 2) + cg * 256);
    int vo[3];
#pragma unroll
    for (int i = 0; i < 3; ++i) { const int n = 64 * i + lane, row = (n * 2731) >> 16, c = n - row * 24; vo[i] = row * (HE * 2) + ((c ^ (row >> 2)) * 16 ^ ((w & 1) * 64)); }
    auto issue1 = [&](int n, int i) __attribute__((always_inline)) {
      dma_1k(sv, l0 + ((n + 4) % 6) * 24576 + w * 6144 + i * 1024, vo[i % 3] ^ ((i / 3) * 32), (n * 32 + (w & 1) * 16 + (i / 3) * 8) * (HE * 2) + h * (E_ * 2));
    };
    auto issue = [&](int n) __attribute__((always_inline)) {
#pragma unroll
      for (int i = 0; i < 6; ++i) issue1(n, i);
    };
    issue(0); issue(1);
    const int srow = 16 * (rho >> 3) + 4 * ((rho >> 2) & 1) + (rho & 1) + 8 * ((rho >> 1) & 1);
    lchar* ar[2];
#pragma unroll
    for (int k = 0; k < 2; ++k) ar[k] = L + (w >> 1) * 12288 + srow * 384 + ((4 * k + g) ^ (srow >> 2)) * 16;
    VMWAIT(12);
    BAR();
    v4u bw[4][6], lo[4];
    {
      const int q = rho >> 2, pp = lane & 3, key = 2 * (q | ((g & 1) << 2));
#pragma unroll
      for (int ct = 0; ct < 4; ++ct) {
        lchar* b = L + (w >> 1) * 49152 + (8 * g + q) * 256 + ((2 * (4 * (w & 1) + ct) + (pp >> 1)) ^ key) * 16 + (pp & 1) * 8;
#pragma unroll
        for (int ks = 0; ks < 6; ++ks) { const v2u x0 = lrtr(b + ks * 8192), x1 = lrtr(b + ks * 8192 + 1024); bw[ct][ks] = v4u{x0.x, x0.y, x1.x, x1.y}; }
      }
    }
    BAR();
    issue(2); issue(3); issue(4); issue(5);
    v4u a0[6], a1[6];
    auto rd = [&](auto nc, auto kc) __attribute__((always_inline)) {
      constexpr int n = decltype(nc)::value, ks = decltype(kc)::value;
      lchar* x = ar[ks & 1] + ((n + 4) % 6) * 24576 + (ks >> 1) * 128;
      a0[ks] = lr16(x); a1[ks] = lr16(x + 2 * 384);
    };
    vmwait<30>();
    BAR();
    static_for<6>([&](auto kc) __attribute__((always_inline)) { rd(IC<0>{}, kc); });
    static_for<20>([&](auto nc) __attribute__((always_inline)) {
      constexpr int n = decltype(nc)::value;
      if constexpr (n < 19) {
        vmwait<((n + 5 < 19 ? n + 5 : 19) - (n + 1)) * 6>();
        BAR();
      }
      tile_pipe<4, false>(a0, a1, bw, [&](auto tc, v4u v) __attribute__((always_inline)) {
        constexpr int t = decltype(tc)::value;
        if constexpr ((n & 1) == 0) lo[t] = v;
        else {
          v8u x = cat(lo[t], v);
          if constexpr (n / 2 < 8) PIN_A(x); else PIN_V(x);
          u[t][n / 2] = x;
        }
      }, [&](auto tc, auto kc) __attribute__((always_inline)) {
        constexpr int t = decltype(tc)::value, ks = decltype(kc)::value;
        if constexpr (n < 19 && t == 3) rd(IC<n + 1>{}, kc);
        if constexpr (n + 6 < 20 && t < 3 && (ks == 0 || ks == 4)) issue1(n + 6, 2 * t + ks / 4);
      });
    });
    BAR();
  }
  constexpr int C_M = 0, C_PC = 10240, PCS = 1024 + 64;
  const int sidx = (lane & 8) ? 0xEEEE : 0x4444;
  const unsigned nm = near_mask(ws);
  const int poff[5] = {lane * 16, 1024 + lane * 16, 2048 + lane * 16, 3072 + lane * 16, 4096 + lane * 16};
  lchar* Lm_own = L + C_M + w * 2048 + lane * 16;
  lchar* Lm_half = L + C_M + 8192 + w * 512 + lane * 8;
  lchar* Lm = L + C_M + lane * 16;
  lchar* Lpc_w = L + C_PC + w * PCS + lane * 16;
  lchar* Lpc_r = L + C_PC + (lane & 3) * PCS + ((lane >> 3) + 16 * ((lane >> 2) & 1)) * 16;
  const int mst = (lane & 7) == 0 ? m_line(r) + (lane >> 3) * 16 : 0x30000000;
  const int flz = OFF_CTRL + C_FLZ + (w * 8 + (lane & 7)) * 128 + (r / 20) * 32, fly = lane == 0 ? OFF_CTRL + C_FLY + r * 128 + w * 32 : 0x30000000;
  yown += (nm >> 2) * 8192;
  const int yst = hs + OFF_YR + (nm >> 2) * 8192 + (cg * 8 + (w & 1) * 4 + g) * 16384 + ((lane >> 3) & 1) * 4096 + h * 128 + (lane & 7) * 16;
  unsigned zprog = 0, tref = 0;
  const int zl = (lane * 16) ^ ((r & 7) * 128);
  v4u yprev = poison;
  VMWAIT(0);
#pragma unroll 1
  for (int s = 0; s < NSEG; ++s) {
    const int hb = hand_base(hs, nm, s);
    const bool more = s + 1 < NSEG;
    v4u yz[5];
    f4 acc[4];
#pragma unroll
    for (int i = 0; i < 4; ++i) {
      f2 a, b;
      asm volatile("v_mov_b64 %0, 0\n\tv_mov_b64 %1, 0" : "=v"(a), "=v"(b));
      acc[i] = f4{a.x, a.y, b.x, b.y};
    }
    {
      v4u v0, v1; v2u v2;
      gather_m(ws, lane, hb + w * 16384 + 5120, v0, v1, v2);
      VMWAIT(0);
      lw16(Lm_own, v0); lw16(Lm_own + 1024, v1); lw8(Lm_half, v2);
    }
    SB();
    if (s >= LATE) st4<SC1>((unsigned)s, ws, fly, 0);
    const bool zlate = __builtin_amdgcn_ballot_w64(zprog < (unsigned)s + 1u) != 0ull;
#pragma unroll
    for (int i = 0; i < 4; ++i) yz[1 + i] = ld16<SC1>(ws, i * 1024 + zl, zlate ? 0x30000000 : OFF_Z + r * ZR + zseg(s) + w * 4096);
    if (__builtin_amdgcn_ballot_w64(zprog < (unsigned)min(s + 4, NSEG)) != 0ull) zprog = ld4<SC1>(ws, flz, 0);
    SB();
    BAR();
    v4u m[10];
#pragma unroll
    for (int t = 0; t < 10; ++t) m[t] = lr16(Lm + t * 1024);
    SB();
    static_for<10>([&](auto tc) __attribute__((always_inline)) {
      constexpr int t = decltype(tc)::value;
      f4 &c0 = acc[0], &c1 = acc[1], &c2 = acc[2], &c3 = acc[3];
      const v4u& mm = m[t];
      const v8u &u0 = u[0][t], &u1 = u[1][t], &u2 = u[2][t], &u3 = u[3][t];
      const int& sidx_ = sidx;
      {
        const int sidx = sidx_;
        if constexpr (t < 8) { SMF_A(c0, mm, u0); SMF_A(c1, mm, u1); SMF_A(c2, mm, u2); SMF_A(c3, mm, u3); }
        else { SMF_V(c0, mm, u0); SMF_V(c1, mm, u1); SMF_V(c2, mm, u2); SMF_V(c3, mm, u3); }
      }
    });
    Ann ann;
    ann_issue(p.ws + hb + 7680, ann);
    NOPS(7);
    SB();
    {
      float X[4], Y[4];
#pragma unroll
      for (int i = 0; i < 4; ++i) {
        const auto a = __builtin_amdgcn_permlane32_swap(fu(acc[0][i]), fu(acc[2][i]), false, false);
        X[i] = uf(a[0]) + uf(a[1]);
        const auto b = __builtin_amdgcn_permlane32_swap(fu(acc[1][i]), fu(acc[3][i]), false, false);
        Y[i] = uf(b[0]) + uf(b[1]);
      }
      const auto q0 = __builtin_amdgcn_permlane16_swap(nosent(pk(X[0], X[1])), nosent(pk(Y[0], Y[1])), false, false);
      const auto q1 = __builtin_amdgcn_permlane16_swap(nosent(pk(X[2], X[3])), nosent(pk(Y[2], Y[3])), false, false);
      st16<SC1>(v4u{q0[0], q1[0], q0[1], q1[1]}, ws, yst, (s & 1) * YRS);
    }
    SB();
    if (s > 0) {
      st16<SC1>(poison, ws, yown, ((s - 1) & 1) * YRS);
      st16<SC1>(yprev, ws, w * 1024 + lane * 16, OFF_Y + r * ZR + zseg(s - 1));
    }
    if (zlate) {
      if (__builtin_amdgcn_ballot_w64(zprog < (unsigned)s + 1u) != 0ull) zprog = wait_flags(ws, flz, (unsigned)s + 1u);
      __builtin_amdgcn_fence(__ATOMIC_ACQUIRE, "wavefront");
#pragma unroll
      for (int i = 0; i < 4; ++i) yz[1 + i] = ld16<SC1>(ws, i * 1024 + zl, OFF_Z + r * ZR + zseg(s) + w * 4096);
    }
    unsigned back;
    if (!ann_latest(ann, clk() + 256u, tref, back)) tref = latest_seen(ws, lane, hb, back);
    if (!more) {
      const int yo[1] = {yown};
      v4u y1[1];
      const v2u xv = *(const GAS v2u*)(p.x + (size_t)(s * 32 + w * 8 + (lane >> 3)) * D_ + 8 * r + ((lane >> 2) & 1) * 4);
      const v2u ga = ld8<0>(ws, (8 * r + ((lane >> 2) & 1) * 4) * 2, OFF_CTRL + C_MOD + 2 * D_ * 2);
      gather<1>(ws, yo, (s & 1) * YRS, y1);
      yz[0] = y1[0];
      nap_until(tref + T_NAP);
      const int pb = hs + OFF_PT + s * 81920 + w * 5120;
      int po[20];
#pragma unroll
      for (int k = 0; k < 20; ++k) po[k] = lane * 16 + (k / 5) * 20480 + (k % 5) * 1024;
      v4u pq[20];
      gather<20>(ws, po, pb, pq);
#pragma unroll
      for (int q = 0; q < 4; ++q) {
        f4 o;
        MF32_0(o, yz[0], pq[5 * q]);
#pragma unroll
        for (int k = 1; k < 5; ++k) MF32_C(o, yz[k], pq[5 * q + k]);
        NOPS(7);
        PIN_V(o);
        lwf4(Lpc_w + q * 4 * PCS, o);
      }
      BAR();
      f4 x = lrf4(Lpc_r + w * 4 * PCS) + lrf4(Lpc_r + w * 4 * PCS + 40 * 16);
#pragma unroll
      for (int i = 0; i < 4; ++i) x[i] += dppf<0xB1>(x[i]);
#pragma unroll
      for (int i = 0; i < 4; ++i) x[i] += dppf<0x4E>(x[i]);
      const unsigned a0 = nosent(pk(blo(xv.x) + blo(ga.x) * x[0], bhi(xv.x) + bhi(ga.x) * x[1])), a1 = nosent(pk(blo(xv.y) + blo(ga.y) * x[2], bhi(xv.y) + bhi(ga.y) * x[3]));
      const unsigned n0 = dppu<0x104>(a0), n1 = dppu<0x104>(a1);
      st16<SC1>(v4u{a0, a1, n0, n1}, ws, (lane & 7) == 0 ? OFF_X1 + (s * 80 + r) * 512 + (w * 8 + (lane >> 3)) * 16 : 0x30000000, 0);
    } else {
      int mst_s = mst + hand_base(hs, nm, s + 1);
      PIN_V(mst_s);
      v4u pf[5];
      nap_until(tref + Y_NAP);
      yz[0] = ld16<SC1>(ws, yown, (s & 1) * YRS);
      SB();
      nap_until(tref + C_NAP);
      f4 o;
      {
        const int so = hb + w * 16384;
        auto attempt = [&]() __attribute__((always_inline)) {
          f4& oo = o;
#pragma unroll
          for (int k = 0; k < 5; ++k) pf[k] = ld16<SC1>(ws, poff[k], so);
          SB();
          unsigned mm = mx4(0u, yz[0]);
          SB();
          { const v4u &ya = yz[0], &pa = pf[0]; MF32_0(oo, ya, pa); }
          mm = mx4(mm, pf[0]);
          static_for<4>([&](auto kc) __attribute__((always_inline)) {
            const v4u &ya = yz[1 + decltype(kc)::value], &pa = pf[1 + decltype(kc)::value];
            f4& o2 = oo;
            SB(); MF32_C(o2, ya, pa); mm = mx4(mm, pa);
          });
          SB();
          VMWAIT(0);
          return __builtin_amdgcn_ballot_w64(mm == 0xFFFFFFFFu);
        };
        if (__builtin_expect(attempt() != 0ull, 0)) {
          int left = SPIN_CAP;
          do {
            asm volatile("s_sub_u32 %0, %0, 1\n\ts_cbranch_scc0 1f\n\ts_endpgm\n1:\n\ts_nop 7" : "+s"(left)::"scc", "memory");
            yz[0] = ld16<SC1>(ws, yown, (s & 1) * YRS);
          } while (attempt() != 0ull);
          NOPS(7);
          PIN_V(o);
        }
      }
      NOPS(3);
      SB();
      lwf4(Lpc_w, o);
      BAR();
      if (w == 0) {
        f4 x = lrf4(Lpc_r) + lrf4(Lpc_r + 40 * 16);
        {
          float x0 = x[0], x1 = x[1], x2 = x[2], x3 = x[3];
#define QADD(q) "v_add_f32_dpp %0, %0, %0 quad_perm:" q " row_mask:0xf bank_mask:0xf\n\tv_add_f32_dpp %1, %1, %1 quad_perm:" q " row_mask:0xf bank_mask:0xf\n\t" \
                "v_add_f32_dpp %2, %2, %2 quad_perm:" q " row_mask:0xf bank_mask:0xf\n\tv_add_f32_dpp %3, %3, %3 quad_perm:" q " row_mask:0xf bank_mask:0xf\n\t"
          asm volatile("s_nop 1\n\t" QADD("[1,0,3,2]") QADD("[2,3,0,1]") : "+v"(x0), "+v"(x1), "+v"(x2), "+v"(x3));
          x = f4{x0, x1, x2, x3};
        }
        const unsigned a0 = nosent(pk(x[0], x[1])), a1 = nosent(pk(x[2], x[3]));
        const unsigned n0 = dppu<0x104>(a0), n1 = dppu<0x104>(a1);
        st16<SC1>(v4u{a0, a1, n0, n1}, ws, mst_s, 0);
      }
    }
    SB();
    yprev = yz[0];
    if (s >= LATE - 1 && more) st16<SC1>(yprev, ws, w * 1024 + lane * 16, OFF_Y + r * ZR + zseg(s));
    if (more) nap_until(tref + back);
  }
}

DEVI void role_z(const Params& p, rsrc_t ws, lchar* L, int w, int h) {
  const int lane = lane_id(), g = lane >> 4;
  v4u wo[10][6];
  {
    const v4u so_ = mk_srd(p.Wo, D_ * HE * 2);
    int vo[10];
#pragma unroll
    for (int i = 0; i < 10; ++i) { const int n = 64 * i + lane, row = n / 80; vo[i] = (n ^ (2 * row)) * 16; }
    const int q = (lane & 15) >> 2, pp = lane & 3, row = 4 * g + q;
    const unsigned lb = (unsigned)(size_t)L + w * 10240;
    auto issue = [&](auto kc) __attribute__((always_inline)) {
      constexpr int ks = decltype(kc)::value;
#pragma unroll
      for (int i = 0; i < 10; ++i) dma_1k(so_, lb + (ks % 3) * 40960 + i * 1024, vo[i], h * E_ * D_ * 2 + ks * 40960 + w * 10240);
    };
    asm volatile("s_nop 4");
    static_for<3>([&](auto kc) __attribute__((always_inline)) { issue(kc); });
    static_for<6>([&](auto kc) __attribute__((always_inline)) {
      constexpr int ks = decltype(kc)::value;
      vmwait<(5 - ks < 2 ? 5 - ks : 2) * 10>();
      BAR();
#pragma unroll
      for (int t = 0; t < 10; ++t) {
        lchar* a = L + (ks % 3) * 40960 + row * 1280 + ((w * 20 + 2 * t + (pp >> 1)) ^ (2 * (row & 7))) * 16 + (pp & 1) * 8;
        const v2u x0 = lrtr(a), x1 = lrtr(a + 16 * 1280);
        wo[t][ks] = v4u{x0.x, x0.y, x1.x, x1.y};
      }
      BAR();
      if constexpr (ks + 3 < 6) issue(IC<ks + 3>{});
      static_for<10>([&](auto tc) __attribute__((always_inline)) {
        constexpr int t = decltype(tc)::value;
        v4u x = wo[t][ks];
        PIN_A(x);
        wo[t][ks] = x;
      });
      SB();
    });
  }
  const int flv = OFF_CTRL + C_FLB + (h * 3 + 2) * 128 + (lane & 3) * 32;
  const int flz = lane == 0 ? OFF_CTRL + C_FLZ + h * 128 + w * 32 : 0x30000000;
  const int zo = OFF_Z + (w * 20 + ((lane >> 3) & 1)) * ZR + (h >> 3) * 4096 + g * 1024 + (((h & 7) ^ ((lane >> 3) & 1)) * 128) + (lane & 7) * 16;
  unsigned vprog = 0, fv = 0;
  bool ready = false;
  v4u vn[12];
#pragma unroll
  for (int k = 0; k < 12; ++k) vn[k] = v4u{0, 0, 0, 0};
#pragma unroll 1
  for (int i = 0; i < NSEG; ++i) {
    if (!ready) {
      if (__builtin_amdgcn_ballot_w64(fv < (unsigned)i + 1u) != 0ull) {
        VMWAIT(0); st4<SC1>((unsigned)i, ws, flz, 0);
        vprog = wait_flags(ws, flv, (unsigned)i + 1u);
      }
      __builtin_amdgcn_fence(__ATOMIC_ACQUIRE, "wavefront");
#pragma unroll
      for (int k = 0; k < 12; ++k) vn[k] = ld16<SC1>(ws, lane * 16, OFF_QK + h * QH + i * QS + 2 * 12288 + k * 1024);
      __builtin_amdgcn_s_waitcnt(0x0F70);
    }
    v4u va[2][6];
#pragma unroll
    for (int k = 0; k < 4; ++k) { va[0][k] = vn[3 * k]; va[1][k] = vn[3 * k + 1]; }
#pragma unroll
    for (int k = 0; k < 2; ++k) {
      va[0][4 + k] = v4u{vn[6 * k + 2].x, vn[6 * k + 2].y, vn[6 * k + 5].x, vn[6 * k + 5].y};
      va[1][4 + k] = v4u{vn[6 * k + 2].z, vn[6 * k + 2].w, vn[6 * k + 5].z, vn[6 * k + 5].w};
    }
#pragma unroll
    for (int k = 0; k < 6; ++k) { PIN_V(va[0][k]); PIN_V(va[1][k]); }
    NOPS(1);
    const int nx = i + 1 < NSEG ? i + 1 : i;
    vprog = max(vprog, fv);
    ready = __builtin_amdgcn_ballot_w64(vprog < (unsigned)nx + 1u) == 0ull;
    __builtin_amdgcn_fence(__ATOMIC_ACQUIRE, "wavefront");
    const int vso = OFF_QK + h * QH + nx * QS + 2 * 12288, zs = zo + zseg(i);
    tile_pipe<10, true>(va[0], va[1], wo, [&](auto tc, v4u v) __attribute__((always_inline)) { st16<SC1>(v, ws, zs ^ (((w * 20 + 2 * decltype(tc)::value) & 7) * 128), decltype(tc)::value * 2 * ZR); },
      [&](auto tc, auto kc) __attribute__((always_inline)) {
        constexpr int n = decltype(kc)::value < 2 ? 2 * decltype(tc)::value + decltype(kc)::value : 99;
        if constexpr (n < 12) vn[n] = ld16<SC1>(ws, lane * 16, vso + n * 1024);
        if constexpr (n == 10) { VMWAIT(15); st4<SC1>((unsigned)i, ws, flz, 0); }
        if constexpr (n == 15) fv = ld4<SC1>(ws, flv, 0);
      });
  }
  VMWAIT(0);
  st4<SC1>((unsigned)NSEG, ws, flz, 0);
}

DEVI void role_d(const Params& p, rsrc_t ws, lchar* L, int w, int q, int sidx_) {
  const int hs = hand_shift(p.ws);
  const int lane = lane_id(), g = lane >> 4;
  GAS unsigned* tk = (GAS unsigned*)(p.ws + OFF_CTRL + C_DTK + q * 128);
  LAS unsigned* Ltk = (LAS unsigned*)(L + 81920);
  const int flz = OFF_CTRL + C_FLZ + (w * 8 + ((lane >> 2) & 7)) * 128 + (lane & 3) * 32;
  const int flw = lane < 16 ? OFF_CTRL + C_FLP + (w * 8 + (lane >> 1)) * 64 + (lane & 1) * 4 : OFF_CTRL + C_FLY + (q * 10 + min(lane - 16, 9)) * 128 + w * 32;
  const int flw0 = OFF_CTRL + C_FLP + w * 8 * 64;
  const int zoff = ((lane >> 3) & 1) * (ZR + 128) + g * 1024 + (lane & 7) * 16, yoff = ((lane >> 3) & 1) * ZR + g * 128 + (lane & 7) * 16;
  const rsrc_t rx = mkrs(p.x, S_ * D_ * 2);
  const int xoff = (lane & 15) * (D_ * 2) + q * 160 + g * 8;
  v2u ga[3];
#pragma unroll
  for (int k = 0; k < 3; ++k) ga[k] = ld8<0>(ws, q * 160 + g * 8, OFF_CTRL + C_MOD + 2 * D_ * 2 + (min(w + 4 * k, 9) >> 1) * 32);
  const int flx = lane == 0 ? OFF_CTRL + C_FLX + q * 16 + w * 4 : 0x30000000;
  unsigned zprog = 0;
  int buf = 0;
  v4u z[5][8];
  v2u xv[3];
  auto fetch = [&](int j, int done) __attribute__((always_inline)) {
    if (__builtin_amdgcn_ballot_w64(zprog < (unsigned)j + 1u) != 0ull) { VMWAIT(0); st4<SC1>(1u, ws, done, 0); zprog = wait_flags(ws, flz, (unsigned)j + 1u); }
    const int zb = __builtin_amdgcn_readfirstlane(OFF_Z + q * 10 * ZR + zseg(j) + w * 4096);
#pragma unroll
    for (int t = 0; t < 5; ++t) {
#pragma unroll
      for (int k = 0; k < 8; ++k) z[t][k] = ld16<SC1>(ws, zoff ^ ((k ^ ((q * 10 + 2 * t) & 7)) * 128), zb + t * 2 * ZR);
      if (t == 1) { SB(); VMWAIT(16); st4<SC1>(1u, ws, done, 0); SB(); }
    }
#pragma unroll
    for (int k = 0; k < 3; ++k) { const int n = min(w + 4 * k, 9); xv[k] = ld8<0>(rx, xoff, (j * 32 + (n & 1) * 16) * (D_ * 2) + (n >> 1) * 32); }
    SB();
  };
  if (w == 0 && lane == 0) Ltk[0] = __hip_atomic_fetch_add(tk, 1u, __ATOMIC_RELAXED, __HIP_MEMORY_SCOPE_AGENT);
  BAR();
  int j = __builtin_amdgcn_readfirstlane(Ltk[0]);
  if (j >= NSEG - 1) return;
  fetch(j, 0x30000000);
#pragma unroll 1
  for (;;) {
    wait_flags_calm(ws, (lane < 16 || j >= LATE - 1) ? flw : flw0, (unsigned)j + 1u);
    unsigned nt = 0;
    if (w == 0 && lane == 0) nt = __hip_atomic_fetch_add(tk, 1u, __ATOMIC_RELAXED, __HIP_MEMORY_SCOPE_AGENT);
    const int yb = __builtin_amdgcn_readfirstlane(OFF_Y + q * 10 * ZR + zseg(j) + w * 1024);
    const int pb = __builtin_amdgcn_readfirstlane(hs + OFF_PT + j * 81920 + w * 8192), pmb = __builtin_amdgcn_readfirstlane(hs + OFF_PT + j * 81920 + 65536 + w * 2048);
    f4 acc[5][2];
    {
      v4u y[5][2], pt[20];
#pragma unroll
      for (int k = 0; k < 16; ++k) pt[k] = ld16<SC1>(ws, lane * 16, pb + (k & 1) * 32768 + (k >> 1) * 1024);
#pragma unroll
      for (int k = 0; k < 4; ++k) pt[16 + k] = ld16<SC1>(ws, lane * 16, pmb + (k & 1) * 8192 + (k >> 1) * 1024);
#pragma unroll
      for (int t = 0; t < 5; ++t) { y[t][0] = ld16<SC1>(ws, yoff, yb + t * 2 * ZR); y[t][1] = ld16<SC1>(ws, yoff + 512, yb + t * 2 * ZR); }
      SB();
#pragma unroll
      for (int t = 0; t < 5; ++t) { acc[t][0] = f4{0, 0, 0, 0}; acc[t][1] = f4{0, 0, 0, 0}; }
#pragma unroll
      for (int k = 0; k < 10; ++k) {
#pragma unroll
        for (int tt = 0; tt < 2; ++tt) {
#pragma unroll
          for (int t = 0; t < 5; ++t) acc[t][tt] = mfma32(k < 8 ? z[t][k] : y[t][k - 8], pt[2 * k + tt], acc[t][tt]);
        }
        SB();
      }
    }
    lchar* pc = L + buf * 40960 + lane * 16;
#pragma unroll
    for (int t = 0; t < 5; ++t) { lwf4(pc + w * 10240 + t * 2048, acc[t][0]); lwf4(pc + w * 10240 + t * 2048 + 1024, acc[t][1]); }
    if (w == 0 && lane == 0) Ltk[1 + buf] = nt;
    BAR();
    const int jn = __builtin_amdgcn_readfirstlane(Ltk[1 + buf]);
#pragma unroll
    for (int k = 0; k < 3; ++k) {
      const int n = min(w + 4 * k, 9);
      const f4 x = (lrf4(pc + n * 1024) + lrf4(pc + 10240 + n * 1024)) + (lrf4(pc + 20480 + n * 1024) + lrf4(pc + 30720 + n * 1024));
      const v2u o = {pk(blo(xv[k].x) + blo(ga[k].x) * x[0], bhi(xv[k].x) + bhi(ga[k].x) * x[1]), pk(blo(xv[k].y) + blo(ga[k].y) * x[2], bhi(xv[k].y) + bhi(ga[k].y) * x[3])};
      st8<SC1>(o, ws, (g >> 1) * 512 + (lane & 15) * 16 + (g & 1) * 8 + OFF_X1 + (j * 80 + q * 10 + (n >> 1) * 2) * 512 + (n & 1) * 256, 0);
    }
    SB();
    if (jn >= NSEG - 1) break;
    fetch(jn, flx + j * 128);
    j = jn;
    buf ^= 1;
  }
  VMWAIT(0);
  st4<SC1>(1u, ws, flx + j * 128, 0);
}

DEVI void role_b(const Params& p, rsrc_t ws, lchar* L, int w, int hw) {
  const int lane = lane_id();
  const v4u srd = mk_srd(p.ws, WS_BYTES);
  const unsigned lx = (unsigned)(size_t)L + w * 10240;
  v4u wq[3][20];
  {
    const int wh = hw % 3;
    const v4u sw = mk_srd(wh == 0 ? p.Wq : wh == 1 ? p.Wk : p.Wv, D_ * HE * 2);
    const int hb = (hw / 3) * (E_ * 2);
    stream_proj<5, false, HE * 2, 0>(L, lane, w, [&](auto, auto) __attribute__((always_inline)) { return sw; },
      [&](auto gc, auto cc) __attribute__((always_inline)) { return (decltype(gc)::value * 4 + decltype(cc)::value) * 32 * (HE * 2) + hb; },
      [&](auto, auto cc) __attribute__((always_inline)) { return decltype(cc)::value; },
      [&](auto gc, auto cc, v4u (&o)[3]) __attribute__((always_inline)) {
        constexpr int ks = decltype(gc)::value * 4 + decltype(cc)::value;
        PIN_A(o[0]); PIN_A(o[1]); PIN_A(o[2]);
        wq[0][ks] = o[0]; wq[1][ks] = o[1]; wq[2][ks] = o[2];
      });
  }
  wait_flags(ws, OFF_CTRL + C_FLH + (lane & 15) * 4, 1u);
  asm volatile("s_nop 4");
#pragma unroll
  for (int k = 0; k < 20; ++k) dma_sc1(srd, lx + (k / 10) * 40960 + (k % 10) * 1024, lane * 16, OFF_HF + (k / 10) * 40960 + w * 10240 + (k % 10) * 1024);
  const int flb = lane == 0 ? OFF_CTRL + C_FLB + hw * 128 + w * 32 : 0x30000000;
  int so = OFF_QK + (hw / 3) * QH + (hw % 3) * 12288 + w * 3072 + lane * 16;
  VMWAIT(10);
  BAR();
  v4u xa[4][2];
  unsigned tpace = 0;
#pragma unroll
  for (int k = 0; k < 2; ++k) { xa[k][0] = lr16(L + lane * 16 + k * 1024); xa[k][1] = lr16(L + lane * 16 + 20480 + k * 1024); }
#pragma unroll 1
  for (int i = 0; i < NSEG; ++i) {
    if (i == 12) wait_flags(ws, OFF_CTRL + C_FLH + lane * 4, 1u);
    const int cur = i & 3, nxt = (i + 1) & 3, nx2 = (i + 2) & 3;
    const int xso = OFF_HF + min(i + 2, NSEG - 1) * 40960 + w * 10240;
    f4 acc[3][2];
    proj_seg(acc, wq, xa, L + cur * 40960 + lane * 16, L + nxt * 40960 + lane * 16, [&](auto kc) __attribute__((always_inline)) {
      constexpr int ks = decltype(kc)::value;
      if constexpr (ks < 10) dma_sc1(srd, lx + nx2 * 40960 + ks * 1024, lane * 16, xso + ks * 1024);
      else if constexpr (ks == B_FLAG) { VMWAIT(10); st4<SC1>((unsigned)i, ws, flb, 0); }
      else if constexpr (ks == 17) { __builtin_amdgcn_s_barrier(); CFENCE(); }
    });
    st16<SC1>(v4u{PK4(acc[0][0]), PK4(acc[1][0])}, ws, so, 0);
    st16<SC1>(v4u{PK4(acc[0][1]), PK4(acc[1][1])}, ws, so, 1024);
    st16<SC1>(v4u{PK4(acc[2][0]), PK4(acc[2][1])}, ws, so, 2048);
    so += QS;
    if (i > B_FREE) nap_until(tpace);
    tpace = clk() + B_PACE - (hw & 7) * B_STAG;
  }
  VMWAIT(0);
  st4<SC1>((unsigned)NSEG, ws, flb, 0);
}

DEVI float gelu_t(float v) {
  constexpr float C1 = -2.8853900817779268f * 0.7978845608028654f, C2 = C1 * 0.044715f;
  return v * __builtin_amdgcn_rcpf(1.f + __builtin_amdgcn_exp2f(v * __builtin_fmaf(v * v, C2, C1)));
}
constexpr int NTEAM = 6;
DEVI void role_m1(const Params& p, rsrc_t ws, lchar* L, int w, int team, int fb) {
  const int lane = lane_id(), g = lane >> 4, tid = w * 64 + lane, row = tid & 15, part = tid >> 4;
  v4u wq[3][20];
  {
    const v4u sw = mk_srd(p.w1, D_ * F_ * 2);
    stream_proj<5, false, F_ * 2, M_GAP>(L, lane, w, [&](auto, auto) __attribute__((always_inline)) { return sw; },
      [&](auto gc, auto cc) __attribute__((always_inline)) { return (decltype(gc)::value * 4 + decltype(cc)::value) * 32 * (F_ * 2) + fb * 384; },
      [&](auto, auto cc) __attribute__((always_inline)) { return decltype(cc)::value; },
      [&](auto gc, auto cc, v4u (&o)[3]) __attribute__((always_inline)) {
        constexpr int ks = decltype(gc)::value * 4 + decltype(cc)::value;
        PIN_A(o[0]); PIN_A(o[1]); PIN_A(o[2]);
        wq[0][ks] = o[0]; wq[1][ks] = o[1]; wq[2][ks] = o[2];
      });
  }
  f4 cs[3], b1[3];
  {
    v4u ones = {0x3F803F80u, 0x3F803F80u, 0x3F803F80u, 0x3F803F80u};
    PIN_V(ones);
    NOPS(1);
    static_for<20>([&](auto kc) __attribute__((always_inline)) {
      constexpr int ks = decltype(kc)::value;
      f4 &c0 = cs[0], &c1 = cs[1], &c2 = cs[2];
      const v4u &w0 = wq[0][ks], &w1 = wq[1][ks], &w2 = wq[2][ks], &on = ones;
      if constexpr (ks == 0) { MF32_0B(c0, w0, on); MF32_0B(c1, w1, on); MF32_0B(c2, w2, on); } else { MF32_CB(c0, w0, on); MF32_CB(c1, w1, on); MF32_CB(c2, w2, on); }
    });
    NOPS(7);
    SB();
  }
#pragma unroll
  for (int t = 0; t < 3; ++t) { const v2u x = *(const GAS v2u*)(p.b1 + fb * 192 + (t < 2 ? 32 * w + 16 * t : 128 + 16 * w) + 4 * g); b1[t] = f4{blo(x.x), bhi(x.x), blo(x.y), bhi(x.y)}; }
  f2 ma[20], mb[20];
#pragma unroll
  for (int k = 0; k < 5; ++k) {
    const v4u sb = ld16<0>(ws, (part + 16 * k) * 16, OFF_CTRL + C_MOD + 3 * D_ * 2), cb = ld16<0>(ws, (part + 16 * k) * 16, OFF_CTRL + C_MOD + 4 * D_ * 2);
#pragma unroll
    for (int i = 0; i < 4; ++i) { ma[4 * k + i] = f2{1.f + blo(cb[i]), 1.f + bhi(cb[i])}; mb[4 * k + i] = f2{blo(sb[i]), bhi(sb[i])}; }
  }
  const int flt = lane == 0 ? OFF_CTRL + C_FLT + (team * 8 + fb) * 128 + w * 32 : 0x30000000;
  LAS f4* red = (LAS f4*)(L + 40960);
  unsigned fx = 0;
#pragma unroll 1
  for (int j = team; j < NSEG; j += NTEAM) {
    const int flx = OFF_CTRL + C_FLX + j * 128 + (lane & 31) * 4;
    if (__builtin_amdgcn_ballot_w64(fx == 0u) != 0ull) {
      VMWAIT(0); st4<SC1>((unsigned)j, ws, flt, 0);
      if (j + 1 < NSEG) wait_flags(ws, flx, 1u);
    }
    __builtin_amdgcn_fence(__ATOMIC_ACQUIRE, "wavefront");
    v4u xv[2][5];
    {
      int left = SPIN_CAP;
      unsigned long long bad;
      do {
        unsigned m = 0;
#pragma unroll
        for (int n = 0; n < 2; ++n)
#pragma unroll
          for (int k = 0; k < 5; ++k) xv[n][k] = ld16<SC1>(ws, part * 512 + row * 16 + n * 256, OFF_X1 + j * 40960 + k * 8192);
#pragma unroll
        for (int n = 0; n < 2; ++n)
#pragma unroll
          for (int k = 0; k < 5; ++k) m = mx4(m, xv[n][k]);
        bad = j + 1 < NSEG ? 0ull : __builtin_amdgcn_ballot_w64(m == 0xFFFFFFFFu);
        asm volatile("s_sub_u32 %0, %0, 1\n\ts_cbranch_scc0 1f\n\ts_endpgm\n1:" : "+s"(left)::"scc", "memory");
      } while (bad != 0ull);
    }
    VMWAIT(0);
    __builtin_amdgcn_s_waitcnt(0x0F70);
    st4<SC1>((unsigned)j, ws, flt, 0);
    ln_stats(xv, red, w, lane);
    BAR();
    const f4 n1 = ln_finish(red, row);
#pragma unroll
    for (int n = 0; n < 2; ++n)
#pragma unroll
      for (int k = 0; k < 5; ++k) {
#pragma unroll
        for (int i = 0; i < 4; ++i) {
          const f2 x = (f2{blo(xv[n][k][i]), bhi(xv[n][k][i])} * n1[2 * n] + n1[2 * n + 1]) * ma[4 * k + i] + mb[4 * k + i];
          xv[n][k][i] = pk(x.x, x.y);
        }
        lw16(L + n * 20480 + (part + 16 * k) * 256 + row * 16, xv[n][k]);
      }
    ln_stats(xv, red + 64, w, lane);
    BAR();
    v4u xa[4][2];
#pragma unroll
    for (int k = 0; k < 2; ++k) { xa[k][0] = lr16(L + lane * 16 + k * 1024); xa[k][1] = lr16(L + lane * 16 + 20480 + k * 1024); }
    const f4 n2 = ln_finish(red + 64, row);
    f4 acc[3][2];
    proj_seg(acc, wq, xa, L + lane * 16, L + lane * 16, [&](auto kc) __attribute__((always_inline)) {
      if constexpr (decltype(kc)::value == 12) fx = ld4<SC1>(ws, flx, NTEAM * 128);
    });
    v2u o[3][2];
#pragma unroll
    for (int t = 0; t < 3; ++t)
#pragma unroll
      for (int tt = 0; tt < 2; ++tt) {
        const f4 u = acc[t][tt] * n2[2 * tt] + (cs[t] * n2[2 * tt + 1] + b1[t]);
        o[t][tt] = v2u{pk(gelu_t(u[0]), gelu_t(u[1])), pk(gelu_t(u[2]), gelu_t(u[3]))};
      }
    PIN_V(fx);
    const int so = OFF_T + j * 98304 + fb * 12288 + w * 3072;
    st16<SC1>(v4u{o[0][0].x, o[0][0].y, o[1][0].x, o[1][0].y}, ws, lane * 16, so);
    st16<SC1>(v4u{o[0][1].x, o[0][1].y, o[1][1].x, o[1][1].y}, ws, lane * 16 + 1024, so);
    st16<SC1>(v4u{o[2][0].x, o[2][0].y, o[2][1].x, o[2][1].y}, ws, lane * 16 + 2048, so);
  }
  VMWAIT(0);
  st4<SC1>((unsigned)NSEG, ws, flt, 0);
}
DEVI void role_m2(const Params& p, rsrc_t ws, lchar* L, int w, int team, int cb) {
  const int lane = lane_id(), g = lane >> 4;
  v4u w2[4][12];
  {
    const v4u sw = mk_srd(p.w2, D_ * F_ * 2);
    const unsigned lb = (unsigned)(size_t)L + w * 24576;
    lchar* ra = L + w * 24576 + (4 * g + ((lane & 15) >> 2)) * 128 + (lane & 3) * 16;
    static_for<2>([&](auto bc) __attribute__((always_inline)) {
      constexpr int b = decltype(bc)::value;
      asm volatile("s_nop 4");
#pragma unroll
      for (int i = 0; i < 24; ++i) dma_1k(sw, lb + i * 1024, (lane >> 3) * (D_ * 2) + (lane & 7) * 16, ((2 * w + b) * 192 + i * 8) * (D_ * 2) + cb * 128), __builtin_amdgcn_s_sleep(M_GAP / 3);
      VMWAIT(0);
      static_for<24>([&](auto nc) __attribute__((always_inline)) {
        constexpr int ct = decltype(nc)::value / 6, ks = decltype(nc)::value % 6;
        lchar* a = ra + ks * 4096 + (ct >> 1) * 64 + (ct & 1) * 8;
        const v2u x0 = lrtr(a), x1 = lrtr(a + 2048);
        v4u x = {x0.x, x0.y, x1.x, x1.y};
        PIN_A(x);
        w2[ct][6 * b + ks] = x;
      });
      SB();
    });
  }
  BAR();
  const int pr = w >> 1, tt = w & 1, col = cb * 64 + pr * 32 + g * 8;
  const v4u b2 = *(const GAS v4u*)(p.b2 + col), gb = ld16<0>(ws, col * 2, OFF_CTRL + C_MOD + 5 * D_ * 2);
  const int flt = OFF_CTRL + C_FLT + (team * 8 + 2 * w + ((lane >> 2) & 1)) * 128 + (lane & 3) * 32;
  int buf = 0;
#pragma unroll 1
  for (int j = team; j < NSEG; j += NTEAM) {
    wait_flags(ws, flt, (unsigned)j + 1u);
    v4u tn[24];
#pragma unroll
    for (int k = 0; k < 24; ++k) tn[k] = ld16<SC1>(ws, lane * 16 + (k & 3) * 1024, OFF_T + j * 98304 + w * 24576 + (k >> 2) * 4096);
    const v4u xv = ld16<SC1>(ws, g * 512 + (lane & 15) * 16, OFF_X1 + j * 40960 + (cb * 8 + pr * 4) * 512 + tt * 256);
    SB();
    f4 acc[4][2];
#pragma unroll
    for (int ct = 0; ct < 4; ++ct) { acc[ct][0] = f4{0, 0, 0, 0}; acc[ct][1] = f4{0, 0, 0, 0}; }
#pragma unroll
    for (int b = 0; b < 2; ++b)
#pragma unroll
      for (int ks = 0; ks < 6; ++ks) {
        const v4u* t = tn + 12 * b;
        const int k = ks - 4;
        const v4u t0 = ks < 4 ? t[3 * ks] : v4u{t[6 * k + 2].x, t[6 * k + 2].y, t[6 * k + 5].x, t[6 * k + 5].y};
        const v4u t1 = ks < 4 ? t[3 * ks + 1] : v4u{t[6 * k + 2].z, t[6 * k + 2].w, t[6 * k + 5].z, t[6 * k + 5].w};
#pragma unroll
        for (int ct = 0; ct < 4; ++ct) { acc[ct][0] = mfma32(w2[ct][6 * b + ks], t0, acc[ct][0]); acc[ct][1] = mfma32(w2[ct][6 * b + ks], t1, acc[ct][1]); }
      }
    lchar* pc = L + buf * 32768 + lane * 16;
#pragma unroll
    for (int ct = 0; ct < 4; ++ct) { lwf4(pc + w * 8192 + ct * 2048, acc[ct][0]); lwf4(pc + w * 8192 + ct * 2048 + 1024, acc[ct][1]); }
    BAR();
    f4 x[2];
#pragma unroll
    for (int q = 0; q < 2; ++q) {
      lchar* a = pc + (2 * pr + q) * 2048 + tt * 1024;
      x[q] = (lrf4(a) + lrf4(a + 8192)) + (lrf4(a + 16384) + lrf4(a + 24576));
    }
    v4u o;
#pragma unroll
    for (int i = 0; i < 4; ++i)
      o[i] = pk(blo(xv[i]) + blo(gb[i]) * (x[i >> 1][2 * (i & 1)] + blo(b2[i])), bhi(xv[i]) + bhi(gb[i]) * (x[i >> 1][2 * (i & 1) + 1] + bhi(b2[i])));
    *(GAS v4u*)(p.out + (size_t)(j * 32 + tt * 16 + (lane & 15)) * D_ + col) = o;
    buf ^= 1;
  }
}

__global__ void __launch_bounds__(256, 1) k3_chain(Params p) {
  extern __shared__ __attribute__((aligned(16))) unsigned char smem[];
  lchar* L = (lchar*)smem;
  const rsrc_t ws = mkrs(p.ws, WS_BYTES);
  if (threadIdx.x == 0) {
    unsigned x = xcc_id(), slot = 0;
    for (int k = 0; k < 8; ++k) {
      const unsigned t = __hip_atomic_fetch_add((GAS unsigned*)(p.ws + OFF_CTRL + C_TICKET + x * 128), 1u, __ATOMIC_RELAXED, __HIP_MEMORY_SCOPE_AGENT);
      if (t < 32u) { slot = x * 32 + t; break; }
      x = (x + 1) & 7u;
    }
    *(LAS unsigned*)L = slot;
  }
  {
    const int wv = __builtin_amdgcn_readfirstlane(threadIdx.x >> 6), ln = threadIdx.x & 63;
    const v4u sr = wv < 2 ? mk_srd(wv ? p.w1 : p.w2, D_ * F_ * 2) : mk_srd(p.ws, WS_BYTES);
    asm volatile("s_nop 4\n\ts_mov_b32 m0, %0\n\ts_nop 0\n\tbuffer_load_dword %1, %2, 0 offen lds" ::"s"((unsigned)(size_t)L + 162816 + wv * 256),
                 "v"(wv < 2 ? ln * 32768 : ((wv - 2) * 64 + ln) * 0x200000 + 61440), "s"(sr) : "memory");
  }
  BAR();
  const int slot = __builtin_amdgcn_readfirstlane(*(LAS unsigned*)L), w = __builtin_amdgcn_readfirstlane(threadIdx.x >> 6);
  BAR();
  const int x = slot >> 5, t = slot & 31;
  const int nc = x < 3 ? 27 : 26, ai = x == 0 ? t : -1, ci = (x - 1) * 27 + t;
  const int bi = __builtin_amdgcn_readfirstlane(x == 0 ? -1 : x < 4 ? (t < nc ? -1 : (x - 1) * 5 + t - nc) : t < 20 ? 16 + (x - 4) * 20 + t : -1);
  const int zi = __builtin_amdgcn_readfirstlane(x >= 4 && t >= 20 && t < 28 ? (x - 4) * 8 + t - 20 : -1);
  const int dq = __builtin_amdgcn_readfirstlane(x >= 4 && t >= 28 ? (x - 4) * 4 + t - 28 : (bi >= 48 && bi < 68) ? bi - 32 : -1);
  const int m2 = __builtin_amdgcn_readfirstlane(zi >= 0 ? 28 + zi : bi - 68);
  const int qk = x < 4 ? bi : 16 + (x - 4) * 12 + t - 8;
  const int hw = __builtin_amdgcn_readfirstlane(x >= 4 && t < 8 ? ((x - 4) * 8 + t) * 3 + 2 : (qk >> 1) * 3 + (qk & 1));
  if (dq >= 0 && dq < 16) {
#pragma unroll 1
    for (int k = dq; k < NSEG; k += 16) h_seg(p, ws, L, w, k);
  }
  if (x >= 4 && t >= 8 && t < 20) nap_until(clk() + QK_DELAY);
  if (ai >= 0) role_a(p, ws, L, w, ai);
  else if (bi >= 0) role_b(p, ws, L, w, hw);
  else if (zi >= 0) role_z(p, ws, L, w, zi);
  else if (dq < 0) role_c1(p, ws, L, w, ci);
  if (bi >= 0 || zi >= 0) {
    VMWAIT(0);
    BAR();
    if (m2 >= 0) role_m2(p, ws, L, w, m2 / 10, m2 % 10);
    else if (bi < 48) role_m1(p, ws, L, w, bi >> 3, bi & 7);
  }
  if (dq >= 16) nap_until(clk() + (dq - 16) * D_STAG);
  if (dq >= 0 && dq < D_MAX) role_d(p, ws, L, w, dq & 7, dq < 16 ? 112 + dq : 400 + dq);
}

}

extern "C" void kernel_launch(void* const* d_in, const int* in_sizes, int n_in, void* d_out, int out_size, void* d_ws, size_t ws_size,
                              hipStream_t stream) {
  Params p;
  p.x = (const u16*)d_in[0]; p.mem0 = (const u16*)d_in[1]; p.Wq = (const u16*)d_in[2]; p.Wk = (const u16*)d_in[3];
  p.Wv = (const u16*)d_in[4]; p.Wo = (const u16*)d_in[5]; p.w1 = (const u16*)d_in[8]; p.b1 = (const u16*)d_in[9];
  p.w2 = (const u16*)d_in[10]; p.b2 = (const u16*)d_in[11]; p.c = (const u16*)d_in[12]; p.wmod = (const u16*)d_in[13];
  p.out = (u16*)d_out;
  p.ws = (unsigned char*)d_ws;
  (void)hipFuncSetAttribute((const void*)k3_chain, hipFuncAttributeMaxDynamicSharedMemorySize, 163840);
  k0_prep<<<272, 256, 0, stream>>>(p);
  k3_chain<<<256, 256, 163840, stream>>>(p);
}
```
